```python
import torch

def _softmax(x, axis=-1):
    return torch.softmax(x, dim=axis)
def _gelu(x):
    return torch.nn.functional.gelu(x, approximate="tanh")
def _silu(x):
    return torch.nn.functional.silu(x)
def _sigmoid(x):
    return torch.sigmoid(x)
def _layernorm(x, axis=-1, eps=1e-5):
    m = x.mean(dim=axis, keepdim=True)
    v = x.var(dim=axis, keepdim=True, unbiased=False)
    return (x - m) / torch.sqrt(v + eps)
def _scatter_add(tgt, idx, src, axis=0):
    return tgt.clone().index_add_(axis, idx, src)
def _segment_sum(src, seg, n):
    out = torch.zeros((n,) + src.shape[1:], dtype=src.dtype)
    return out.index_add_(0, seg, src)
def _pad(x, pw):
    flat = tuple(v for pair in reversed(pw) for v in pair)
    return torch.nn.functional.pad(x, flat)

kernel_name = 'gated_mix__grouped_kv_mqa__sliding_local_dilated__prompt'

def setup_inputs():

    _w = (lambda a: (lambda t: t.to(torch.bfloat16) if t.is_floating_point() else t)(torch.as_tensor(a)))
    def _A_setup():
        B, S, D, H = 1, 2048, 448, 16
        Dh = D // H
        return {
            "x": _w((lambda *s: torch.randn(*s, dtype=torch.bfloat16) * 0.05)(B, S, D)),
            "wq": _w((lambda *s: torch.randn(*s, dtype=torch.bfloat16) * 1.6)(D, H * Dh)),
            "wk": _w((lambda *s: torch.randn(*s, dtype=torch.bfloat16) * 1.6)(D, Dh)),
            "wv": _w((lambda *s: torch.randn(*s, dtype=torch.bfloat16) * 0.2)(D, Dh)),
            "wo": _w((lambda *s: torch.randn(*s, dtype=torch.bfloat16) * 0.2)(H * Dh, D)),
        }
    _A_in = _A_setup()

    def _B_setup():
        B, S, D = 1, 2048, 448
        return {
            "x": _w((lambda *s: torch.randn(*s, dtype=torch.bfloat16) * 0.05)(B, S, D)),
            "wqkv": _w((lambda *s: torch.randn(*s, dtype=torch.bfloat16) * 0.8)(D, 3 * D)),
            "wo": _w((lambda *s: torch.randn(*s, dtype=torch.bfloat16) * 0.05)(D, D)),
        }
    _B_in = _B_setup()

    return {
        "A_x": _A_in["x"],
        "A_wq": _A_in["wq"],
        "A_wk": _A_in["wk"],
        "A_wv": _A_in["wv"],
        "A_wo": _A_in["wo"],
        "B_x": _B_in["x"],
        "B_wqkv": _B_in["wqkv"],
        "B_wo": _B_in["wo"],
        "gate_w": _w((lambda *s: torch.randn(*s, dtype=torch.bfloat16) * 0.05)(448) * 16.0),
    }

def reference(A_x, A_wq, A_wk, A_wv, A_wo, B_x, B_wqkv, B_wo, gate_w):
    def _A_ref(x, wq, wk, wv, wo):
        B, S, D = x.shape
        H = 16
        Dh = D // H
        q = (lambda x, *d: x.permute(*d))((x @ wq).reshape(B, S, H, Dh), 0, 2, 1, 3)
        k = x @ wk
        v = x @ wv
        scores = torch.einsum("bhqd,bkd->bhqk", q, k) * (Dh ** -0.5)
        p = _softmax(scores, axis=-1)
        o = torch.einsum("bhqk,bkd->bhqd", p, v)
        return (lambda x, *d: x.permute(*d))(o, 0, 2, 1, 3).reshape(B, S, H * Dh) @ wo

    def _B_ref(x, wqkv, wo):
        B, S, D = x.shape
        H = 16; Dh = D // H
        W, dil = 8, 2
        qkv = (lambda x, *d: x.permute(*d))((x @ wqkv).reshape(B, S, 3, H, Dh), 2, 0, 3, 1, 4)
        q, k, v = qkv[0], qkv[1], qkv[2]
        scale = Dh ** -0.5
        pos = torch.arange(S)
        off = torch.arange(W) * dil
        idx = pos[:, None] - off[None, :]
        mask = idx >= 0
        idx_c = torch.where(mask, idx, 0).reshape(-1)
        kg = k[:, :, idx_c, :].reshape(B, H, S, W, Dh)
        vg = v[:, :, idx_c, :].reshape(B, H, S, W, Dh)
        logits = torch.einsum("bhsd,bhswd->bhsw", q, kg) * scale
        logits = torch.where(mask[None, None, :, :], logits, -1e9)
        attn = _softmax(logits, axis=-1)
        o = torch.einsum("bhsw,bhswd->bhsd", attn, vg)
        return (lambda x, *d: x.permute(*d))(o, 0, 2, 1, 3).reshape(B, S, D) @ wo

    x = A_x
    g = _sigmoid((x * gate_w).sum(-1, keepdims=True))
    a = _A_ref(x, A_wq, A_wk, A_wv, A_wo)
    b = _B_ref(x, B_wqkv, B_wo)
    return g * a + (1 - g) * b
```

```cpp
#include <hip/hip_runtime.h>
#include <hip/hip_bf16.h>
#include <stdint.h>

typedef __bf16 bf16_t;
typedef __bf16 bf16x8 __attribute__((ext_vector_type(8)));
typedef __bf16 bf16x4 __attribute__((ext_vector_type(4)));
typedef __bf16 bf16x2 __attribute__((ext_vector_type(2)));
typedef short s16x4 __attribute__((ext_vector_type(4)));
typedef float f32x16 __attribute__((ext_vector_type(16)));
typedef float f32x2 __attribute__((ext_vector_type(2)));
typedef float f32x4 __attribute__((ext_vector_type(4)));
typedef unsigned u32x2 __attribute__((ext_vector_type(2)));
typedef unsigned u32x4 __attribute__((ext_vector_type(4)));

#define LDS_AS __attribute__((address_space(3)))
#define GLB_AS __attribute__((address_space(1)))

constexpr int S_ = 2048, D_ = 448, H_ = 16, DH_ = 28;
constexpr int NQKV = 3 * D_;

constexpr size_t WS_QP = 0;
constexpr size_t WS_KF = WS_QP + (size_t)H_ * S_ * 32 * 2;
constexpr size_t WS_VF = WS_KF + (size_t)S_ * 32 * 2;
constexpr size_t WS_QKVB = WS_VF + (size_t)S_ * 32 * 2;
constexpr size_t WS_OAB = WS_QKVB + (size_t)S_ * NQKV * 2;
constexpr size_t WS_G = WS_OAB + (size_t)S_ * 896 * 2;
constexpr size_t WS_END = WS_G + (size_t)S_ * 4;

__device__ __forceinline__ void glds16(const void* gsrc, void* lds_dst) {
  __builtin_amdgcn_global_load_lds((const GLB_AS unsigned*)gsrc, (LDS_AS unsigned*)lds_dst, 16, 0, 0);
}
#define WAIT_VM(n) asm volatile("s_waitcnt vmcnt(" #n ")" ::: "memory")
#define WAIT_LGKM0() asm volatile("s_waitcnt lgkmcnt(0)" ::: "memory")
__device__ __forceinline__ void barrier_raw() {
  asm volatile("" ::: "memory");
  __builtin_amdgcn_s_barrier();
  asm volatile("" ::: "memory");
}
__device__ __forceinline__ bf16x8 lds_ld16(const char* p) { return *(const bf16x8*)p; }
__device__ __forceinline__ s16x4 lds_tr(const char* p) {
  return __builtin_amdgcn_ds_read_tr16_b64_v4i16((LDS_AS s16x4*)p);
}
__device__ __forceinline__ bf16x8 cat_tr(s16x4 a, s16x4 b) {
  typedef short s16x8 __attribute__((ext_vector_type(8)));
  s16x8 r = {a[0], a[1], a[2], a[3], b[0], b[1], b[2], b[3]};
  return __builtin_bit_cast(bf16x8, r);
}
__device__ __forceinline__ unsigned pk2(float a, float b) {
  f32x2 v = {a, b};
  bf16x2 r = __builtin_convertvector(v, bf16x2);
  return __builtin_bit_cast(unsigned, r);
}
__device__ __forceinline__ float rbf(float a) { return (float)(__bf16)a; }
__device__ __forceinline__ float bf2f(unsigned short u) { return __uint_as_float(((unsigned)u) << 16); }
__device__ __forceinline__ float bflo(unsigned u) { return __uint_as_float(u << 16); }
__device__ __forceinline__ float bfhi(unsigned u) { return __uint_as_float(u & 0xffff0000u); }
#define MFMA32(a, b, c) __builtin_amdgcn_mfma_f32_32x32x16_bf16((a), (b), (c), 0, 0, 0)

constexpr int K1_STAGE = 32768;
constexpr int K1_LDS = 4 * K1_STAGE;

__global__ __launch_bounds__(512) void k1_proj(const bf16_t* __restrict__ x, const bf16_t* __restrict__ wq,
                                               const bf16_t* __restrict__ wk, const bf16_t* __restrict__ wv,
                                               const bf16_t* __restrict__ wqkv, const bf16_t* __restrict__ gate_w,
                                               char* __restrict__ ws) {
  extern __shared__ __attribute__((aligned(16))) char smem[];
  const int tid = threadIdx.x, lane = tid & 63, w = __builtin_amdgcn_readfirstlane(tid >> 6);
  const int r = lane & 31, h = lane >> 5;
  const int bid = blockIdx.x;
  if (bid < 224) {
    const int mt = bid / 14, nt = bid % 14;
    const int m0 = mt * 128, n0 = nt * 128;
    const bf16_t* xsrc[2];
    const bf16_t* wsrc[2];
#pragma unroll
    for (int i = 0; i < 2; ++i) {
      int row = (w * 2 + i) * 8 + (lane >> 3), slot = lane & 7;
      xsrc[i] = x + (size_t)(m0 + row) * D_ + ((slot ^ ((row >> 1) & 7)) * 8);
      int krow = (w * 2 + i) * 4 + (lane >> 4), wslot = lane & 15;
      int vcol = n0 + ((wslot ^ ((krow & 3) << 2)) * 8);
      if (n0 < 448 && vcol < 448) wsrc[i] = wq + (size_t)krow * 448 + vcol;
      else wsrc[i] = wqkv + (size_t)krow * NQKV + (vcol - 448);
    }
    int wstr[2];
#pragma unroll
    for (int i = 0; i < 2; ++i) {
      int krow = (w * 2 + i) * 4 + (lane >> 4), wslot = lane & 15;
      int vcol = n0 + ((wslot ^ ((krow & 3) << 2)) * 8);
      wstr[i] = (vcol < 448) ? 448 * 64 : NQKV * 64;
    }
    auto issue = [&](int s) {
      char* base = smem + (s & 3) * K1_STAGE;
#pragma unroll
      for (int i = 0; i < 2; ++i) {
        glds16(xsrc[i] + s * 64, base + (w * 2 + i) * 1024);
        glds16(wsrc[i] + (size_t)s * wstr[i], base + 16384 + (w * 2 + i) * 1024);
      }
    };
    issue(0); issue(1); issue(2);
    const int wn = w & 3, wm = w >> 2;
    f32x16 acc0 = {}, acc1 = {};
    const int xrow0 = wm * 64 + r, xrow1 = wm * 64 + 32 + r;
    const int g16 = lane >> 4, q4 = (lane & 15) >> 2, p4 = lane & 3;
    const int trh = g16 >> 1, trb = g16 & 1;
    const int wchunk = wn * 4 + trb * 2 + (p4 >> 1);
#pragma unroll
    for (int s = 0; s < 7; ++s) {
      if (s < 4) WAIT_VM(8); else if (s == 4) WAIT_VM(8); else if (s == 5) WAIT_VM(4); else WAIT_VM(0);
      barrier_raw();
      if (s + 3 < 7) issue(s + 3);
      const char* xb = smem + (s & 3) * K1_STAGE;
      const char* wb = xb + 16384;
#pragma unroll
      for (int ks = 0; ks < 4; ++ks) {
        bf16x8 b0 = lds_ld16(xb + xrow0 * 128 + (((2 * ks + h) ^ ((xrow0 >> 1) & 7)) * 16));
        bf16x8 b1 = lds_ld16(xb + xrow1 * 128 + (((2 * ks + h) ^ ((xrow1 >> 1) & 7)) * 16));
        int k0 = ks * 16 + trh * 8 + q4;
        s16x4 a0 = lds_tr(wb + k0 * 256 + ((wchunk ^ ((k0 & 3) << 2)) * 16) + (p4 & 1) * 8);
        s16x4 a1 = lds_tr(wb + (k0 + 4) * 256 + ((wchunk ^ ((k0 & 3) << 2)) * 16) + (p4 & 1) * 8);
        bf16x8 a = cat_tr(a0, a1);
        acc0 = MFMA32(a, b0, acc0);
        acc1 = MFMA32(a, b1, acc1);
      }
    }
    const int nb = n0 + wn * 32;
    bf16_t* qp = (bf16_t*)(ws + WS_QP);
    bf16_t* qkvb = (bf16_t*)(ws + WS_QKVB);
#pragma unroll
    for (int mi = 0; mi < 2; ++mi) {
      const int m = m0 + wm * 64 + mi * 32 + r;
      const f32x16& acc = mi ? acc1 : acc0;
#pragma unroll
      for (int g = 0; g < 4; ++g) {
        const int n = nb + 8 * g + 4 * h;
        u32x2 v = {pk2(acc[4 * g], acc[4 * g + 1]), pk2(acc[4 * g + 2], acc[4 * g + 3])};
        if (nb < 448) {
          const int head = n / 28, d = n - head * 28;
          bf16_t* dst = qp + ((size_t)(head * S_ + m) * 32 + d);
          *(u32x2*)dst = v;
          if (d == 24) *(u32x2*)(dst + 4) = (u32x2){0u, 0u};
        } else {
          *(u32x2*)(qkvb + (size_t)m * NQKV + (n - 448)) = v;
        }
      }
    }
  } else {
    const int j = bid - 224, m0 = j * 64;
    char* xl = smem;
    char* wl = smem + 64 * 896;
#pragma unroll
    for (int i = 0; i < 7; ++i) {
      int sl = (w * 7 + i) * 64 + lane;
      int row = sl / 56, slot = sl - row * 56;
      glds16(x + (size_t)(m0 + row) * D_ + ((slot ^ ((row >> 1) & 7)) * 8), xl + (w * 7 + i) * 1024);
    }
    for (int idx = tid; idx < 448 * 16; idx += 512) {
      int row = idx >> 4, pc = idx & 15;
      u32x2 v = {0u, 0u};
      if (pc < 7) v = *(const u32x2*)(wk + row * 28 + pc * 4);
      else if (pc >= 8 && pc < 15) v = *(const u32x2*)(wv + row * 28 + (pc - 8) * 4);
      int chunk = pc >> 1;
      *(u32x2*)(wl + row * 128 + ((chunk ^ (((row >> 1) & 1) << 2)) * 16) + (pc & 1) * 8) = v;
    }
    WAIT_VM(0);
    WAIT_LGKM0();
    barrier_raw();
    if (w < 4) {
      const int wn = w & 1, wm = w >> 1;
      const int xrow = wm * 32 + r;
      const int g16 = lane >> 4, q4 = (lane & 15) >> 2, p4 = lane & 3;
      const int trh = g16 >> 1, trb = g16 & 1;
      const int wchunk = wn * 4 + trb * 2 + (p4 >> 1);
      f32x16 acc = {};
#pragma unroll 4
      for (int ks = 0; ks < 28; ++ks) {
        bf16x8 b = lds_ld16(xl + xrow * 896 + (((2 * ks + h) ^ ((xrow >> 1) & 7)) * 16));
        int k0 = ks * 16 + trh * 8 + q4;
        s16x4 a0 = lds_tr(wl + k0 * 128 + ((wchunk ^ (((k0 >> 1) & 1) << 2)) * 16) + (p4 & 1) * 8);
        s16x4 a1 = lds_tr(wl + (k0 + 4) * 128 + ((wchunk ^ (((k0 >> 1) & 1) << 2)) * 16) + (p4 & 1) * 8);
        acc = MFMA32(cat_tr(a0, a1), b, acc);
      }
      const int key = m0 + wm * 32 + r;
      const int t = key >> 5;
      if (wn == 0) {
        bf16_t* kf = (bf16_t*)(ws + WS_KF);
#pragma unroll
        for (int g = 0; g < 4; ++g) {
          u32x2 v = {pk2(acc[4 * g], acc[4 * g + 1]), pk2(acc[4 * g + 2], acc[4 * g + 3])};
          *(u32x2*)(kf + ((size_t)((t * 2 + (g >> 1)) * 64 + (g & 1) * 32 + r) * 8 + 4 * h)) = v;
        }
      } else {
        bf16_t* vf = (bf16_t*)(ws + WS_VF);
        const int st = r >> 4, jj = ((r >> 3) & 1) * 4 + (r & 3), hh = (r >> 2) & 1;
#pragma unroll
        for (int g = 0; g < 4; ++g) {
#pragma unroll
          for (int e = 0; e < 4; ++e) {
            const int d = 8 * g + 4 * h + e;
            float val = acc[4 * g + e];
            if (d == 28) val = 1.0f;
            vf[(size_t)((t * 2 + st) * 64 + hh * 32 + d) * 8 + jj] = (__bf16)val;
          }
        }
      }
    } else {
      const int t2 = tid - 256, row = t2 >> 2, qq = t2 & 3;
      const bf16_t* xr = x + (size_t)(m0 + row) * D_ + qq * 112;
      const bf16_t* gw = gate_w + qq * 112;
      float sum = 0.f;
#pragma unroll 2
      for (int c = 0; c < 14; ++c) {
        u32x4 xv = *(const u32x4*)(xr + c * 8);
        u32x4 gv = *(const u32x4*)(gw + c * 8);
#pragma unroll
        for (int e = 0; e < 4; ++e) {
          sum += rbf(bflo(xv[e]) * bflo(gv[e]));
          sum += rbf(bfhi(xv[e]) * bfhi(gv[e]));
        }
      }
      sum += __shfl_xor(sum, 1);
      sum += __shfl_xor(sum, 2);
      if (qq == 0) {
        float z = rbf(sum);
        float gsig = rbf(1.0f / (1.0f + __expf(-z)));
        ((float*)(ws + WS_G))[m0 + row] = gsig;
      }
    }
  }
}

constexpr int K2_STAGE = 32768;
constexpr int K2_LDS = 4 * K2_STAGE;
constexpr float SCALE_ = 0.1889822365046136f;
constexpr float CL2 = SCALE_ * 1.4426950408889634f;
constexpr float THR_RAW = 8.0f / SCALE_;

__global__ __launch_bounds__(512) void k2_attn(char* __restrict__ ws) {
  extern __shared__ __attribute__((aligned(16))) char smem[];
  const int tid = threadIdx.x, lane = tid & 63, w = __builtin_amdgcn_readfirstlane(tid >> 6);
  const int r = lane & 31, h = lane >> 5;
  const int bid = blockIdx.x;
  bf16_t* oab = (bf16_t*)(ws + WS_OAB);
  {
    const int head = bid & 15, pb = bid >> 4;
    const int qg = w & 3, kh = w >> 2;
    const bf16_t* kf = (const bf16_t*)(ws + WS_KF);
    const bf16_t* vf = (const bf16_t*)(ws + WS_VF);
    const bf16_t* src[4];
#pragma unroll
    for (int i = 0; i < 4; ++i) {
      int idx = w * 4 + i;
      int kv = idx >> 4, khh = (idx >> 3) & 1, pc = idx & 7;
      src[i] = (kv ? vf : kf) + ((size_t)(khh * 32 * 2 + pc) * 64 + lane) * 8;
    }
    auto issue = [&](int s) {
      char* base = smem + (s & 3) * K2_STAGE + w * 4096;
#pragma unroll
      for (int i = 0; i < 4; ++i) glds16(src[i] + (size_t)s * 8 * 512, base + i * 1024);
    };
    issue(0); issue(1); issue(2);
    const bf16_t* qp = (const bf16_t*)(ws + WS_QP) + ((size_t)(head * S_ + pb * 128 + qg * 32 + r) * 32 + 8 * h);
    const bf16x8 q0 = *(const bf16x8*)qp;
    const bf16x8 q1 = *(const bf16x8*)(qp + 16);
    {
      const int pos = bid * 8 + w, bh = lane >> 2, qd = lane & 3;
      const bool has1 = qd < 3;
      const int c0 = qd * 4, c1 = has1 ? (qd + 4) * 4 : qd * 4;
      const bf16_t* qkvb = (const bf16_t*)(ws + WS_QKVB);
      const bf16_t* qr = qkvb + (size_t)pos * NQKV + bh * 28;
      u32x2 qa = *(const u32x2*)(qr + c0), qb = *(const u32x2*)(qr + c1);
      if (!has1) qb = (u32x2){0u, 0u};
      u32x2 ka[8], kb2[8], va[8], vb2[8];
#pragma unroll
      for (int ww = 0; ww < 8; ++ww) {
        int kp = pos - 2 * ww;
        kp = kp < 0 ? 0 : kp;
        const bf16_t* kr = qkvb + (size_t)kp * NQKV + 448 + bh * 28;
        ka[ww] = *(const u32x2*)(kr + c0); kb2[ww] = *(const u32x2*)(kr + c1);
        va[ww] = *(const u32x2*)(kr + 448 + c0); vb2[ww] = *(const u32x2*)(kr + 448 + c1);
      }
      float lg[8];
#pragma unroll
      for (int ww = 0; ww < 8; ++ww) {
        float acc = bflo(qa[0]) * bflo(ka[ww][0]);
        acc = fmaf(bfhi(qa[0]), bfhi(ka[ww][0]), acc);
        acc = fmaf(bflo(qa[1]), bflo(ka[ww][1]), acc);
        acc = fmaf(bfhi(qa[1]), bfhi(ka[ww][1]), acc);
        acc = fmaf(bflo(qb[0]), bflo(kb2[ww][0]), acc);
        acc = fmaf(bfhi(qb[0]), bfhi(kb2[ww][0]), acc);
        acc = fmaf(bflo(qb[1]), bflo(kb2[ww][1]), acc);
        acc = fmaf(bfhi(qb[1]), bfhi(kb2[ww][1]), acc);
        acc += __shfl_xor(acc, 1);
        acc += __shfl_xor(acc, 2);
        float lv = rbf(rbf(acc) * SCALE_);
        lg[ww] = (pos - 2 * ww) < 0 ? rbf(-1e9f) : lv;
      }
      float mx = lg[0];
#pragma unroll
      for (int ww = 1; ww < 8; ++ww) mx = fmaxf(mx, lg[ww]);
      float sum = 0.f;
#pragma unroll
      for (int ww = 0; ww < 8; ++ww) { lg[ww] = __expf(lg[ww] - mx); sum += lg[ww]; }
      const float inv = 1.0f / sum;
      float oa[4] = {0.f, 0.f, 0.f, 0.f}, ob[4] = {0.f, 0.f, 0.f, 0.f};
#pragma unroll
      for (int ww = 0; ww < 8; ++ww) {
        const float p = rbf(lg[ww] * inv);
        oa[0] = fmaf(p, bflo(va[ww][0]), oa[0]); oa[1] = fmaf(p, bfhi(va[ww][0]), oa[1]);
        oa[2] = fmaf(p, bflo(va[ww][1]), oa[2]); oa[3] = fmaf(p, bfhi(va[ww][1]), oa[3]);
        ob[0] = fmaf(p, bflo(vb2[ww][0]), ob[0]); ob[1] = fmaf(p, bfhi(vb2[ww][0]), ob[1]);
        ob[2] = fmaf(p, bflo(vb2[ww][1]), ob[2]); ob[3] = fmaf(p, bfhi(vb2[ww][1]), ob[3]);
      }
      bf16_t* dst = oab + (size_t)pos * 896 + 448 + bh * 28;
      *(u32x2*)(dst + c0) = (u32x2){pk2(oa[0], oa[1]), pk2(oa[2], oa[3])};
      if (has1) *(u32x2*)(dst + c1) = (u32x2){pk2(ob[0], ob[1]), pk2(ob[2], ob[3])};
    }
    f32x16 o = {};
    float m = -1e30f;
#pragma unroll
    for (int s = 0; s < 8; ++s) {
      if (s < 6) WAIT_VM(8); else if (s == 6) WAIT_VM(4); else WAIT_VM(0);
      barrier_raw();
      if (s + 3 < 8) issue(s + 3);
      const char* kb = smem + (s & 3) * K2_STAGE + kh * 8192 + lane * 16;
      const char* vb = kb + 16384;
#pragma unroll
      for (int it = 0; it < 2; ++it) {
        bf16x8 k00 = lds_ld16(kb + it * 4096), k01 = lds_ld16(kb + it * 4096 + 1024);
        bf16x8 k10 = lds_ld16(kb + it * 4096 + 2048), k11 = lds_ld16(kb + it * 4096 + 3072);
        f32x16 s0 = {}, s1 = {};
        s0 = MFMA32(k00, q0, s0); s1 = MFMA32(k10, q0, s1);
        s0 = MFMA32(k01, q1, s0); s1 = MFMA32(k11, q1, s1);
        bf16x8 v00 = lds_ld16(vb + it * 4096), v01 = lds_ld16(vb + it * 4096 + 1024);
        bf16x8 v10 = lds_ld16(vb + it * 4096 + 2048), v11 = lds_ld16(vb + it * 4096 + 3072);
        float pmax = fmaxf(s0[0], s1[0]);
#pragma unroll
        for (int i = 1; i < 16; ++i) pmax = fmaxf(pmax, fmaxf(s0[i], s1[i]));
        {
          auto rr = __builtin_amdgcn_permlane32_swap(__float_as_uint(pmax), __float_as_uint(pmax), false, false);
          pmax = fmaxf(__uint_as_float(rr[0]), __uint_as_float(rr[1]));
        }
        if (!__all(pmax - m <= THR_RAW)) {
          float mn = fmaxf(m, pmax);
          float alpha = __builtin_amdgcn_exp2f((m - mn) * CL2);
          m = mn;
#pragma unroll
          for (int i = 0; i < 16; ++i) o[i] *= alpha;
        }
        const float mc = -m * CL2;
#pragma unroll
        for (int i = 0; i < 16; ++i) {
          s0[i] = __builtin_amdgcn_exp2f(fmaf(s0[i], CL2, mc));
          s1[i] = __builtin_amdgcn_exp2f(fmaf(s1[i], CL2, mc));
        }
        u32x4 p00 = {pk2(s0[0], s0[1]), pk2(s0[2], s0[3]), pk2(s0[4], s0[5]), pk2(s0[6], s0[7])};
        u32x4 p01 = {pk2(s0[8], s0[9]), pk2(s0[10], s0[11]), pk2(s0[12], s0[13]), pk2(s0[14], s0[15])};
        u32x4 p10 = {pk2(s1[0], s1[1]), pk2(s1[2], s1[3]), pk2(s1[4], s1[5]), pk2(s1[6], s1[7])};
        u32x4 p11 = {pk2(s1[8], s1[9]), pk2(s1[10], s1[11]), pk2(s1[12], s1[13]), pk2(s1[14], s1[15])};
        o = MFMA32(v00, __builtin_bit_cast(bf16x8, p00), o);
        o = MFMA32(v01, __builtin_bit_cast(bf16x8, p01), o);
        o = MFMA32(v10, __builtin_bit_cast(bf16x8, p10), o);
        o = MFMA32(v11, __builtin_bit_cast(bf16x8, p11), o);
      }
    }
    barrier_raw();
    float* red = (float*)smem + qg * (17 * 64);
    if (kh == 1) {
      red[16 * 64 + lane] = m;
#pragma unroll
      for (int i = 0; i < 16; ++i) red[i * 64 + lane] = o[i];
    }
    WAIT_LGKM0();
    barrier_raw();
    if (kh == 0) {
      float m1 = red[16 * 64 + lane];
      float mn = fmaxf(m, m1);
      float a0 = __builtin_amdgcn_exp2f((m - mn) * CL2), a1 = __builtin_amdgcn_exp2f((m1 - mn) * CL2);
#pragma unroll
      for (int i = 0; i < 16; ++i) o[i] = o[i] * a0 + red[i * 64 + lane] * a1;
      float l = __shfl(o[12], 32 + r);
      float inv = 1.0f / l;
      bf16_t* dst = oab + (size_t)(pb * 128 + qg * 32 + r) * 896 + head * 28 + 4 * h;
#pragma unroll
      for (int g = 0; g < 4; ++g) {
        if (g == 3 && h == 1) continue;
        u32x2 v = {pk2(o[4 * g] * inv, o[4 * g + 1] * inv), pk2(o[4 * g + 2] * inv, o[4 * g + 3] * inv)};
        *(u32x2*)(dst + 8 * g) = v;
      }
    }
  }
}

constexpr int K3_STAGE = 32768;
constexpr int K3_LDS = 4 * K3_STAGE;

__global__ __launch_bounds__(512) void k3_out(const bf16_t* __restrict__ woa, const bf16_t* __restrict__ wob,
                                              const char* __restrict__ ws, bf16_t* __restrict__ out) {
  extern __shared__ __attribute__((aligned(16))) char smem[];
  const int tid = threadIdx.x, lane = tid & 63, w = __builtin_amdgcn_readfirstlane(tid >> 6);
  const int r = lane & 31, h = lane >> 5;
  const int bid = blockIdx.x;
  const int mt = bid / 7, nt = bid % 7;
  const int m0 = mt * 64, n0 = nt * 64;
  const int br = w >> 2, wl = w & 3;
  const bf16_t* oab = (const bf16_t*)(ws + WS_OAB);
  const bf16_t* wo = br ? wob : woa;
  const bf16_t* osrc[2];
  const bf16_t* wsrc[2];
#pragma unroll
  for (int i = 0; i < 2; ++i) {
    int row = (wl * 2 + i) * 8 + (lane >> 3), slot = lane & 7;
    osrc[i] = oab + (size_t)(m0 + row) * 896 + br * 448 + ((slot ^ ((row >> 1) & 7)) * 8);
    wsrc[i] = wo + (size_t)row * 448 + n0 + ((slot ^ (((row >> 1) & 1) << 2)) * 8);
  }
  auto issue = [&](int s) {
    char* base = smem + (s & 3) * K3_STAGE + br * 16384;
#pragma unroll
    for (int i = 0; i < 2; ++i) {
      glds16(osrc[i] + s * 64, base + (wl * 2 + i) * 1024);
      glds16(wsrc[i] + (size_t)s * 64 * 448, base + 8192 + (wl * 2 + i) * 1024);
    }
  };
  issue(0); issue(1); issue(2);
  const int wn = wl & 1, wm = wl >> 1;
  const int orow = wm * 32 + r;
  const int g16 = lane >> 4, q4 = (lane & 15) >> 2, p4 = lane & 3;
  const int trh = g16 >> 1, trb = g16 & 1;
  const int wchunk = wn * 4 + trb * 2 + (p4 >> 1);
  f32x16 acc = {};
#pragma unroll
  for (int s = 0; s < 7; ++s) {
    if (s < 5) WAIT_VM(8); else if (s == 5) WAIT_VM(4); else WAIT_VM(0);
    barrier_raw();
    if (s + 3 < 7) issue(s + 3);
    const char* ob = smem + (s & 3) * K3_STAGE + br * 16384;
    const char* wb = ob + 8192;
#pragma unroll
    for (int ks = 0; ks < 4; ++ks) {
      bf16x8 b = lds_ld16(ob + orow * 128 + (((2 * ks + h) ^ ((orow >> 1) & 7)) * 16));
      int k0 = ks * 16 + trh * 8 + q4;
      s16x4 a0 = lds_tr(wb + k0 * 128 + ((wchunk ^ (((k0 >> 1) & 1) << 2)) * 16) + (p4 & 1) * 8);
      s16x4 a1 = lds_tr(wb + (k0 + 4) * 128 + ((wchunk ^ (((k0 >> 1) & 1) << 2)) * 16) + (p4 & 1) * 8);
      acc = MFMA32(cat_tr(a0, a1), b, acc);
    }
  }
  barrier_raw();
  float* red = (float*)smem + wl * (16 * 64);
  if (br == 1) {
#pragma unroll
    for (int i = 0; i < 16; ++i) red[i * 64 + lane] = acc[i];
  }
  WAIT_LGKM0();
  barrier_raw();
  if (br == 0) {
    const int m = m0 + orow;
    const float g = ((const float*)(ws + WS_G))[m];
    const float omg = rbf(1.0f - g);
#pragma unroll
    for (int gq = 0; gq < 4; ++gq) {
      float res[4];
#pragma unroll
      for (int e = 0; e < 4; ++e) {
        float a = rbf(acc[4 * gq + e]);
        float b = rbf(red[(4 * gq + e) * 64 + lane]);
        res[e] = rbf(g * a) + rbf(omg * b);
      }
      u32x2 v = {pk2(res[0], res[1]), pk2(res[2], res[3])};
      *(u32x2*)(out + (size_t)m * D_ + n0 + wn * 32 + 8 * gq + 4 * h) = v;
    }
  }
}

extern "C" void kernel_launch(void* const* d_in, const int* in_sizes, int n_in, void* d_out, int out_size, void* d_ws,
                              size_t ws_size, hipStream_t stream) {
  const bf16_t* x = (const bf16_t*)d_in[0];
  const bf16_t* wq = (const bf16_t*)d_in[1];
  const bf16_t* wk = (const bf16_t*)d_in[2];
  const bf16_t* wv = (const bf16_t*)d_in[3];
  const bf16_t* woa = (const bf16_t*)d_in[4];
  const bf16_t* wqkv = (const bf16_t*)d_in[6];
  const bf16_t* wob = (const bf16_t*)d_in[7];
  const bf16_t* gw = (const bf16_t*)d_in[8];
  char* ws = (char*)d_ws;
  (void)hipFuncSetAttribute((const void*)k1_proj, hipFuncAttributeMaxDynamicSharedMemorySize, K1_LDS);
  (void)hipFuncSetAttribute((const void*)k2_attn, hipFuncAttributeMaxDynamicSharedMemorySize, K2_LDS);
  (void)hipFuncSetAttribute((const void*)k3_out, hipFuncAttributeMaxDynamicSharedMemorySize, K3_LDS);
  hipLaunchKernelGGL(k1_proj, dim3(256), dim3(512), K1_LDS, stream, x, wq, wk, wv, wqkv, gw, ws);
  hipLaunchKernelGGL(k2_attn, dim3(256), dim3(512), K2_LDS, stream, ws);
  hipLaunchKernelGGL(k3_out, dim3(224), dim3(512), K3_LDS, stream, woa, wob, (const char*)ws, (bf16_t*)d_out);
}
```

```cpp
#include <hip/hip_runtime.h>
#include <hip/hip_bf16.h>
#include <stdint.h>

typedef __bf16 bf16_t;
typedef __bf16 bf16x8 __attribute__((ext_vector_type(8)));
typedef __bf16 bf16x4 __attribute__((ext_vector_type(4)));
typedef __bf16 bf16x2 __attribute__((ext_vector_type(2)));
typedef short s16x4 __attribute__((ext_vector_type(4)));
typedef float f32x16 __attribute__((ext_vector_type(16)));
typedef float f32x2 __attribute__((ext_vector_type(2)));
typedef float f32x4 __attribute__((ext_vector_type(4)));
typedef unsigned u32x2 __attribute__((ext_vector_type(2)));
typedef unsigned u32x4 __attribute__((ext_vector_type(4)));

#define LDS_AS __attribute__((address_space(3)))
#define GLB_AS __attribute__((address_space(1)))

constexpr int S_ = 2048, D_ = 448, H_ = 16, DH_ = 28;
constexpr int NQKV = 3 * D_;

constexpr int NP = 1792;
constexpr size_t WS_P = 0;
constexpr size_t WS_KF = WS_P + (size_t)S_ * NP * 2;
constexpr size_t WS_VF = WS_KF + (size_t)S_ * 32 * 2;
constexpr size_t WS_OAB = WS_VF + (size_t)S_ * 32 * 2;
constexpr size_t WS_G = WS_OAB + (size_t)S_ * 896 * 2;
constexpr size_t WS_END = WS_G + (size_t)S_ * 4;

__device__ __forceinline__ void glds16(const void* gsrc, unsigned lds_dst) {
  unsigned keep;
  asm volatile("s_mov_b32 %0, m0\n\ts_mov_b32 m0, %2\n\ts_nop 0\n\tglobal_load_lds_dwordx4 %1, off\n\ts_mov_b32 m0, %0"
               : "=&s"(keep) : "v"(gsrc), "s"(lds_dst) : "memory");
}
__device__ __forceinline__ unsigned lds_off(const void* p) { return (unsigned)(size_t)(const LDS_AS char*)p; }
#define WAIT_VM(n) asm volatile("s_waitcnt vmcnt(" #n ")" ::: "memory")
#define WAIT_LGKM0() asm volatile("s_waitcnt lgkmcnt(0)" ::: "memory")
__device__ __forceinline__ void barrier_raw() {
  asm volatile("" ::: "memory");
  __builtin_amdgcn_s_barrier();
  asm volatile("" ::: "memory");
}
__device__ __forceinline__ bf16x8 lds_ld16(const char* p) { return *(const bf16x8*)p; }
__device__ __forceinline__ s16x4 lds_tr(const char* p) {
  return __builtin_amdgcn_ds_read_tr16_b64_v4i16((LDS_AS s16x4*)p);
}
__device__ __forceinline__ bf16x8 cat_tr(s16x4 a, s16x4 b) {
  typedef short s16x8 __attribute__((ext_vector_type(8)));
  s16x8 r = {a[0], a[1], a[2], a[3], b[0], b[1], b[2], b[3]};
  return __builtin_bit_cast(bf16x8, r);
}
__device__ __forceinline__ unsigned pk2(float a, float b) {
  f32x2 v = {a, b};
  bf16x2 r = __builtin_convertvector(v, bf16x2);
  return __builtin_bit_cast(unsigned, r);
}
__device__ __forceinline__ float rbf(float a) { return (float)(__bf16)a; }
__device__ __forceinline__ float bf2f(unsigned short u) { return __uint_as_float(((unsigned)u) << 16); }
__device__ __forceinline__ float bflo(unsigned u) { return __uint_as_float(u << 16); }
__device__ __forceinline__ float bfhi(unsigned u) { return __uint_as_float(u & 0xffff0000u); }
#define MFMA32(a, b, c) __builtin_amdgcn_mfma_f32_32x32x16_bf16((a), (b), (c), 0, 0, 0)

constexpr int K1_STAGE = 32768;
constexpr int K1_LDS = 4 * K1_STAGE;

__global__ __launch_bounds__(512) void k1_proj(const bf16_t* __restrict__ x, const bf16_t* __restrict__ wq,
                                               const bf16_t* __restrict__ wk, const bf16_t* __restrict__ wv,
                                               const bf16_t* __restrict__ wqkv, const bf16_t* __restrict__ gate_w,
                                               char* __restrict__ ws) {
  extern __shared__ __attribute__((aligned(16))) char smem[];
  const int tid = threadIdx.x, lane = tid & 63, w = __builtin_amdgcn_readfirstlane(tid >> 6);
  const int r = lane & 31, h = lane >> 5;
  const int bid = blockIdx.x;
  if (bid < 224) {
    const int xcd = bid & 7, idx = bid >> 3;
    const int mt = (xcd >> 1) * 4 + idx / 7, nt = (xcd & 1) * 7 + idx % 7;
    const int m0 = mt * 128, n0 = nt * 128;
    const bf16_t* xsrc[2];
    const bf16_t* wsrc[2];
#pragma unroll
    for (int i = 0; i < 2; ++i) {
      int row = (w * 2 + i) * 8 + (lane >> 3), slot = lane & 7;
      xsrc[i] = x + (size_t)(m0 + row) * D_ + ((slot ^ ((row >> 1) & 7)) * 8);
      int krow = (w * 2 + i) * 4 + (lane >> 4), wslot = lane & 15;
      int vcol = n0 + ((wslot ^ ((krow & 3) << 2)) * 8);
      if (n0 < 448 && vcol < 448) wsrc[i] = wq + (size_t)krow * 448 + vcol;
      else wsrc[i] = wqkv + (size_t)krow * NQKV + (vcol - 448);
    }
    int wstr[2];
#pragma unroll
    for (int i = 0; i < 2; ++i) {
      int krow = (w * 2 + i) * 4 + (lane >> 4), wslot = lane & 15;
      int vcol = n0 + ((wslot ^ ((krow & 3) << 2)) * 8);
      wstr[i] = (vcol < 448) ? 448 * 64 : NQKV * 64;
    }
    auto issue = [&](int s) {
      const unsigned base = lds_off(smem) + (s & 3) * K1_STAGE;
#pragma unroll
      for (int i = 0; i < 2; ++i) {
        glds16(xsrc[i] + s * 64, base + (w * 2 + i) * 1024);
        glds16(wsrc[i] + (size_t)s * wstr[i], base + 16384 + (w * 2 + i) * 1024);
      }
    };
    issue(0); issue(1); issue(2);
    const int wn = w & 3, wm = w >> 2;
    f32x16 acc0 = {}, acc1 = {};
    const int xrow0 = wm * 64 + r, xrow1 = wm * 64 + 32 + r;
    const int g16 = lane >> 4, q4 = (lane & 15) >> 2, p4 = lane & 3;
    const int trh = g16 >> 1, trb = g16 & 1;
    const int wchunk = wn * 4 + trb * 2 + (p4 >> 1);
#pragma unroll
    for (int s = 0; s < 7; ++s) {
      if (s < 4) WAIT_VM(8); else if (s == 4) WAIT_VM(8); else if (s == 5) WAIT_VM(4); else WAIT_VM(0);
      barrier_raw();
      if (s + 3 < 7) issue(s + 3);
      const char* xb = smem + (s & 3) * K1_STAGE;
      const char* wb = xb + 16384;
#pragma unroll
      for (int ks = 0; ks < 4; ++ks) {
        bf16x8 b0 = lds_ld16(xb + xrow0 * 128 + (((2 * ks + h) ^ ((xrow0 >> 1) & 7)) * 16));
        bf16x8 b1 = lds_ld16(xb + xrow1 * 128 + (((2 * ks + h) ^ ((xrow1 >> 1) & 7)) * 16));
        int k0 = ks * 16 + trh * 8 + q4;
        s16x4 a0 = lds_tr(wb + k0 * 256 + ((wchunk ^ ((k0 & 3) << 2)) * 16) + (p4 & 1) * 8);
        s16x4 a1 = lds_tr(wb + (k0 + 4) * 256 + ((wchunk ^ ((k0 & 3) << 2)) * 16) + (p4 & 1) * 8);
        bf16x8 a = cat_tr(a0, a1);
        acc0 = MFMA32(a, b0, acc0);
        acc1 = MFMA32(a, b1, acc1);
      }
    }
    barrier_raw();
#pragma unroll
    for (int mi = 0; mi < 2; ++mi) {
      const int ml = wm * 64 + mi * 32 + r;
      const f32x16& acc = mi ? acc1 : acc0;
#pragma unroll
      for (int g = 0; g < 4; ++g) {
        u32x2 v = {pk2(acc[4 * g], acc[4 * g + 1]), pk2(acc[4 * g + 2], acc[4 * g + 3])};
        *(u32x2*)(smem + ml * 272 + (wn * 32 + 8 * g + 4 * h) * 2) = v;
      }
    }
    WAIT_LGKM0();
    barrier_raw();
    bf16_t* P = (bf16_t*)(ws + WS_P);
#pragma unroll
    for (int i = 0; i < 4; ++i) {
      const int ml = w * 16 + i * 4 + (lane >> 4), c = lane & 15;
      u32x4 v = *(const u32x4*)(smem + ml * 272 + c * 16);
      *(u32x4*)(P + (size_t)(m0 + ml) * NP + n0 + c * 8) = v;
    }
  } else {
    const int jb = bid - 224, xcd = jb & 7;
    const int j = (xcd >> 1) * 8 + (xcd & 1) * 4 + (jb >> 3), m0 = j * 64;
    char* xl = smem;
    char* wl = smem + 64 * 896;
#pragma unroll
    for (int i = 0; i < 7; ++i) {
      int sl = (w * 7 + i) * 64 + lane;
      int row = sl / 56, slot = sl - row * 56;
      glds16(x + (size_t)(m0 + row) * D_ + ((slot ^ ((row >> 1) & 7)) * 8), lds_off(xl) + (w * 7 + i) * 1024);
    }
    {
      u32x2 wv_[14];
      const int pc = tid & 15;
      const bf16_t* wsrc = (pc < 8 ? wk : wv) + ((pc & 7) < 7 ? (pc & 7) * 4 : 0);
#pragma unroll
      for (int i = 0; i < 14; ++i) {
        int row = (tid >> 4) + i * 32;
        wv_[i] = *(const u32x2*)(wsrc + row * 28);
      }
#pragma unroll
      for (int i = 0; i < 14; ++i) {
        int row = (tid >> 4) + i * 32;
        u32x2 v = ((pc & 7) < 7) ? wv_[i] : (u32x2){0u, 0u};
        int chunk = pc >> 1;
        *(u32x2*)(wl + row * 128 + ((chunk ^ (((row >> 1) & 1) << 2)) * 16) + (pc & 1) * 8) = v;
      }
    }
    WAIT_VM(0);
    WAIT_LGKM0();
    barrier_raw();
    if (w < 4) {
      const int wn = w & 1, wm = w >> 1;
      const int xrow = wm * 32 + r;
      const int g16 = lane >> 4, q4 = (lane & 15) >> 2, p4 = lane & 3;
      const int trh = g16 >> 1, trb = g16 & 1;
      const int wchunk = wn * 4 + trb * 2 + (p4 >> 1);
      f32x16 acc = {};
#pragma unroll 4
      for (int ks = 0; ks < 28; ++ks) {
        bf16x8 b = lds_ld16(xl + xrow * 896 + (((2 * ks + h) ^ ((xrow >> 1) & 7)) * 16));
        int k0 = ks * 16 + trh * 8 + q4;
        s16x4 a0 = lds_tr(wl + k0 * 128 + ((wchunk ^ (((k0 >> 1) & 1) << 2)) * 16) + (p4 & 1) * 8);
        s16x4 a1 = lds_tr(wl + (k0 + 4) * 128 + ((wchunk ^ (((k0 >> 1) & 1) << 2)) * 16) + (p4 & 1) * 8);
        acc = MFMA32(cat_tr(a0, a1), b, acc);
      }
      const int key = m0 + wm * 32 + r;
      const int t = key >> 5;
      if (wn == 0) {
        bf16_t* kf = (bf16_t*)(ws + WS_KF);
#pragma unroll
        for (int g = 0; g < 4; ++g) {
          u32x2 v = {pk2(acc[4 * g], acc[4 * g + 1]), pk2(acc[4 * g + 2], acc[4 * g + 3])};
          *(u32x2*)(kf + ((size_t)((t * 2 + (g >> 1)) * 64 + (g & 1) * 32 + r) * 8 + 4 * h)) = v;
        }
      } else {
        bf16_t* vt = (bf16_t*)(smem + 64 * 896 + 448 * 128) + wm * 1024;
        const int st = r >> 4, jj = ((r >> 3) & 1) * 4 + (r & 3), hh = (r >> 2) & 1;
#pragma unroll
        for (int g = 0; g < 4; ++g) {
#pragma unroll
          for (int e = 0; e < 4; ++e) {
            const int d = 8 * g + 4 * h + e;
            float val = acc[4 * g + e];
            if (d == 28) val = 1.0f;
            vt[(st * 64 + hh * 32 + d) * 8 + jj] = (__bf16)val;
          }
        }
        WAIT_LGKM0();
        bf16_t* vf = (bf16_t*)(ws + WS_VF) + (size_t)t * 1024;
#pragma unroll
        for (int i = 0; i < 2; ++i) *(u32x4*)(vf + (i * 64 + lane) * 8) = *(const u32x4*)(vt + (i * 64 + lane) * 8);
      }
    } else {
      const int t2 = tid - 256, row = t2 >> 2, qq = t2 & 3;
      const bf16_t* xr = x + (size_t)(m0 + row) * D_ + qq * 112;
      const bf16_t* gw = gate_w + qq * 112;
      float sum = 0.f;
#pragma unroll 2
      for (int c = 0; c < 14; ++c) {
        u32x4 xv = *(const u32x4*)(xr + c * 8);
        u32x4 gv = *(const u32x4*)(gw + c * 8);
#pragma unroll
        for (int e = 0; e < 4; ++e) {
          sum += rbf(bflo(xv[e]) * bflo(gv[e]));
          sum += rbf(bfhi(xv[e]) * bfhi(gv[e]));
        }
      }
      sum += __shfl_xor(sum, 1);
      sum += __shfl_xor(sum, 2);
      if (qq == 0) {
        float z = rbf(sum);
        float gsig = rbf(1.0f / (1.0f + __expf(-z)));
        ((float*)(ws + WS_G))[m0 + row] = gsig;
      }
    }
  }
}

constexpr int K2_STAGE = 32768;
constexpr int K2_BOFF = 3 * K2_STAGE;
constexpr int K2_LDS = K2_BOFF + 46 * 1024;
constexpr float SCALE_ = 0.1889822365046136f;
constexpr float CL2 = SCALE_ * 1.4426950408889634f;
constexpr float THR_RAW = 8.0f / SCALE_;

__global__ __launch_bounds__(1024) void k2_attn(char* __restrict__ ws) {
  extern __shared__ __attribute__((aligned(16))) char smem[];
  const int tid = threadIdx.x, lane = tid & 63, w = __builtin_amdgcn_readfirstlane(tid >> 6);
  const int r = lane & 31, h = lane >> 5;
  const int pos0 = blockIdx.x * 8;
  const int qg = w & 3, kq = w >> 2;
  const bf16_t* P = (const bf16_t*)(ws + WS_P);
  const bf16_t* kf = (const bf16_t*)(ws + WS_KF);
  const bf16_t* vf = (const bf16_t*)(ws + WS_VF);
  const unsigned lbase = lds_off(smem);
#pragma unroll
  for (int i = 0; i < 3; ++i) {
    int p = w * 3 + i;
    p = p > 45 ? 45 : p;
    const bf16_t* src;
    if (p < 39) {
      int o = p * 1024 + lane * 16;
      o = o < 39424 ? o : 0;
      int row = o / 1792, col = o - row * 1792;
      int prow = pos0 - 14 + row;
      prow = prow < 0 ? 0 : prow;
      src = P + (size_t)prow * NP + 896 + (col >> 1);
    } else {
      int o = (p - 39) * 1024 + lane * 16;
      int row = o / 896, col = o - row * 896;
      src = P + (size_t)(pos0 + row) * NP + 448 + (col >> 1);
    }
    glds16(src, lbase + K2_BOFF + p * 1024);
  }
  const bf16_t* src0;
  {
    int pc = (w & 3) * 2;
    src0 = ((pc & 4) ? vf : kf) + ((size_t)((w >> 2) * 16 * 2 + (pc & 3)) * 64 + lane) * 8;
  }
  auto issue = [&](int s) {
    const unsigned base = lbase + (s % 3) * K2_STAGE + w * 2048;
    glds16(src0 + (size_t)s * 4 * 512, base);
    glds16(src0 + (size_t)s * 4 * 512 + 512, base + 1024);
  };
  issue(0); issue(1);
  bf16x8 q0, q1;
  {
    const int qi = qg * 32 + r;
    const bf16_t* qp = P + (size_t)(pos0 + (qi & 7)) * NP + (qi >> 3) * 28 + 8 * h;
    u32x2 a = *(const u32x2*)qp, b = *(const u32x2*)(qp + 4), c = *(const u32x2*)(qp + 16), d = *(const u32x2*)(qp + (h ? 16 : 20));
    if (h) d = (u32x2){0u, 0u};
    u32x4 t0 = {a[0], a[1], b[0], b[1]}, t1 = {c[0], c[1], d[0], d[1]};
    q0 = __builtin_bit_cast(bf16x8, t0); q1 = __builtin_bit_cast(bf16x8, t1);
  }
  f32x16 o = {};
  float m = -1e30f;
#pragma unroll
  for (int s = 0; s < 8; ++s) {
    if (s < 7) WAIT_VM(2); else WAIT_VM(0);
    barrier_raw();
    if (s + 2 < 8) issue(s + 2);
    const char* kb = smem + (s % 3) * K2_STAGE + kq * 8192 + lane * 16;
    const char* vb = kb + 4096;
    bf16x8 k00 = lds_ld16(kb), k01 = lds_ld16(kb + 1024);
    bf16x8 k10 = lds_ld16(kb + 2048), k11 = lds_ld16(kb + 3072);
    f32x16 s0 = {}, s1 = {};
    s0 = MFMA32(k00, q0, s0); s1 = MFMA32(k10, q0, s1);
    s0 = MFMA32(k01, q1, s0); s1 = MFMA32(k11, q1, s1);
    bf16x8 v00 = lds_ld16(vb), v01 = lds_ld16(vb + 1024);
    bf16x8 v10 = lds_ld16(vb + 2048), v11 = lds_ld16(vb + 3072);
    float pmax = m;
#pragma unroll
    for (int i = 0; i < 16; i += 2) pmax = fmaxf(fmaxf(pmax, s0[i]), s0[i + 1]);
#pragma unroll
    for (int i = 0; i < 16; i += 2) pmax = fmaxf(fmaxf(pmax, s1[i]), s1[i + 1]);
    {
      auto rr = __builtin_amdgcn_permlane32_swap(__float_as_uint(pmax), __float_as_uint(pmax), false, false);
      pmax = fmaxf(fmaxf(pmax, __uint_as_float(rr[0])), __uint_as_float(rr[1]));
    }
    if (!__all(pmax - m <= THR_RAW)) {
      float alpha = __builtin_amdgcn_exp2f((m - pmax) * CL2);
      m = pmax;
#pragma unroll
      for (int i = 0; i < 16; ++i) o[i] *= alpha;
    }
    const float mc = -m * CL2;
#pragma unroll
    for (int i = 0; i < 16; ++i) {
      s0[i] = __builtin_amdgcn_exp2f(fmaf(s0[i], CL2, mc));
      s1[i] = __builtin_amdgcn_exp2f(fmaf(s1[i], CL2, mc));
    }
    u32x4 p00 = {pk2(s0[0], s0[1]), pk2(s0[2], s0[3]), pk2(s0[4], s0[5]), pk2(s0[6], s0[7])};
    u32x4 p01 = {pk2(s0[8], s0[9]), pk2(s0[10], s0[11]), pk2(s0[12], s0[13]), pk2(s0[14], s0[15])};
    u32x4 p10 = {pk2(s1[0], s1[1]), pk2(s1[2], s1[3]), pk2(s1[4], s1[5]), pk2(s1[6], s1[7])};
    u32x4 p11 = {pk2(s1[8], s1[9]), pk2(s1[10], s1[11]), pk2(s1[12], s1[13]), pk2(s1[14], s1[15])};
    o = MFMA32(v00, __builtin_bit_cast(bf16x8, p00), o);
    o = MFMA32(v01, __builtin_bit_cast(bf16x8, p01), o);
    o = MFMA32(v10, __builtin_bit_cast(bf16x8, p10), o);
    o = MFMA32(v11, __builtin_bit_cast(bf16x8, p11), o);
  }
  barrier_raw();
  char* outt = smem + 65536;
  if (kq != 0) {
    float* red = (float*)smem + ((kq - 1) * 4 + qg) * (17 * 64);
    red[16 * 64 + lane] = m;
#pragma unroll
    for (int i = 0; i < 16; ++i) red[i * 64 + lane] = o[i];
  }
  {
    const int pl = w >> 1, hd = (w & 1) * 8 + (lane >> 3), c = lane & 7;
    const bool valid = c < 7;
    const int co = hd * 56 + (valid ? c : 0) * 8;
    const char* bb = smem + K2_BOFF;
    u32x2 qv = *(const u32x2*)(bb + 39 * 1024 + pl * 896 + co);
    if (!valid) qv = (u32x2){0u, 0u};
    float lg[8];
#pragma unroll
    for (int ww = 0; ww < 8; ++ww) {
      u32x2 kv = *(const u32x2*)(bb + (14 + pl - 2 * ww) * 1792 + co);
      float acc = bflo(qv[0]) * bflo(kv[0]);
      acc = fmaf(bfhi(qv[0]), bfhi(kv[0]), acc);
      acc = fmaf(bflo(qv[1]), bflo(kv[1]), acc);
      acc = fmaf(bfhi(qv[1]), bfhi(kv[1]), acc);
      acc += __shfl_xor(acc, 1);
      acc += __shfl_xor(acc, 2);
      acc += __shfl_xor(acc, 4);
      float lv = rbf(rbf(acc) * SCALE_);
      lg[ww] = (pos0 + pl - 2 * ww) < 0 ? rbf(-1e9f) : lv;
    }
    float mx = lg[0];
#pragma unroll
    for (int ww = 1; ww < 8; ++ww) mx = fmaxf(mx, lg[ww]);
    float sum = 0.f;
#pragma unroll
    for (int ww = 0; ww < 8; ++ww) { lg[ww] = __expf(lg[ww] - mx); sum += lg[ww]; }
    const float inv = 1.0f / sum;
    float oa[4] = {0.f, 0.f, 0.f, 0.f};
#pragma unroll
    for (int ww = 0; ww < 8; ++ww) {
      const float p = rbf(lg[ww] * inv);
      u32x2 vv = *(const u32x2*)(bb + (14 + pl - 2 * ww) * 1792 + 896 + co);
      oa[0] = fmaf(p, bflo(vv[0]), oa[0]); oa[1] = fmaf(p, bfhi(vv[0]), oa[1]);
      oa[2] = fmaf(p, bflo(vv[1]), oa[2]); oa[3] = fmaf(p, bfhi(vv[1]), oa[3]);
    }
    if (valid) *(u32x2*)(outt + pl * 1792 + 896 + co) = (u32x2){pk2(oa[0], oa[1]), pk2(oa[2], oa[3])};
  }
  WAIT_LGKM0();
  barrier_raw();
  if (kq == 0) {
    float mj[3], mn = m;
#pragma unroll
    for (int j = 0; j < 3; ++j) { mj[j] = ((float*)smem)[(j * 4 + qg) * (17 * 64) + 16 * 64 + lane]; mn = fmaxf(mn, mj[j]); }
    const float a0 = __builtin_amdgcn_exp2f((m - mn) * CL2);
#pragma unroll
    for (int i = 0; i < 16; ++i) o[i] *= a0;
#pragma unroll
    for (int j = 0; j < 3; ++j) {
      const float aj = __builtin_amdgcn_exp2f((mj[j] - mn) * CL2);
      const float* red = (const float*)smem + (j * 4 + qg) * (17 * 64);
#pragma unroll
      for (int i = 0; i < 16; ++i) o[i] = fmaf(red[i * 64 + lane], aj, o[i]);
    }
    const float l = __shfl(o[12], 32 + r);
    const float inv = 1.0f / l;
    const int qi = qg * 32 + r;
    char* dst = outt + (qi & 7) * 1792 + (qi >> 3) * 56 + 8 * h;
#pragma unroll
    for (int g = 0; g < 4; ++g) {
      if (g == 3 && h == 1) continue;
      u32x2 v = {pk2(o[4 * g] * inv, o[4 * g + 1] * inv), pk2(o[4 * g + 2] * inv, o[4 * g + 3] * inv)};
      *(u32x2*)(dst + 16 * g) = v;
    }
  }
  WAIT_LGKM0();
  barrier_raw();
  if (w < 14) {
    u32x4 v = *(const u32x4*)(outt + w * 1024 + lane * 16);
    *(u32x4*)(ws + WS_OAB + (size_t)pos0 * 1792 + w * 1024 + lane * 16) = v;
  }
}

constexpr int K3_STAGE = 32768;
constexpr int K3_LDS = 4 * K3_STAGE;

__global__ __launch_bounds__(512) void k3_out(const bf16_t* __restrict__ woa, const bf16_t* __restrict__ wob,
                                              const char* __restrict__ ws, bf16_t* __restrict__ out) {
  extern __shared__ __attribute__((aligned(16))) char smem[];
  const int tid = threadIdx.x, lane = tid & 63, w = __builtin_amdgcn_readfirstlane(tid >> 6);
  const int r = lane & 31, h = lane >> 5;
  const int bid = blockIdx.x;
  const int xcd = bid & 7, idx = bid >> 3;
  const int mt = xcd * 4 + idx / 7, nt = idx % 7;
  const int m0 = mt * 64, n0 = nt * 64;
  const int br = w >> 2, wl = w & 3;
  const bf16_t* oab = (const bf16_t*)(ws + WS_OAB);
  const bf16_t* wo = br ? wob : woa;
  const bf16_t* osrc[2];
  const bf16_t* wsrc[2];
#pragma unroll
  for (int i = 0; i < 2; ++i) {
    int row = (wl * 2 + i) * 8 + (lane >> 3), slot = lane & 7;
    osrc[i] = oab + (size_t)(m0 + row) * 896 + br * 448 + ((slot ^ ((row >> 1) & 7)) * 8);
    wsrc[i] = wo + (size_t)row * 448 + n0 + ((slot ^ (((row >> 1) & 1) << 2)) * 8);
  }
  auto issue = [&](int s) {
    const unsigned base = lds_off(smem) + (s & 3) * K3_STAGE + br * 16384;
#pragma unroll
    for (int i = 0; i < 2; ++i) {
      glds16(osrc[i] + s * 64, base + (wl * 2 + i) * 1024);
      glds16(wsrc[i] + (size_t)s * 64 * 448, base + 8192 + (wl * 2 + i) * 1024);
    }
  };
  issue(0); issue(1); issue(2);
  const int wn = wl & 1, wm = wl >> 1;
  const int orow = wm * 32 + r;
  const int g16 = lane >> 4, q4 = (lane & 15) >> 2, p4 = lane & 3;
  const int trh = g16 >> 1, trb = g16 & 1;
  const int wchunk = wn * 4 + trb * 2 + (p4 >> 1);
  f32x16 acc = {};
#pragma unroll
  for (int s = 0; s < 7; ++s) {
    if (s < 5) WAIT_VM(8); else if (s == 5) WAIT_VM(4); else WAIT_VM(0);
    barrier_raw();
    if (s + 3 < 7) issue(s + 3);
    const char* ob = smem + (s & 3) * K3_STAGE + br * 16384;
    const char* wb = ob + 8192;
#pragma unroll
    for (int ks = 0; ks < 4; ++ks) {
      bf16x8 b = lds_ld16(ob + orow * 128 + (((2 * ks + h) ^ ((orow >> 1) & 7)) * 16));
      int k0 = ks * 16 + trh * 8 + q4;
      s16x4 a0 = lds_tr(wb + k0 * 128 + ((wchunk ^ (((k0 >> 1) & 1) << 2)) * 16) + (p4 & 1) * 8);
      s16x4 a1 = lds_tr(wb + (k0 + 4) * 128 + ((wchunk ^ (((k0 >> 1) & 1) << 2)) * 16) + (p4 & 1) * 8);
      acc = MFMA32(cat_tr(a0, a1), b, acc);
    }
  }
  barrier_raw();
  float* red = (float*)smem + wl * (16 * 64);
  if (br == 1) {
#pragma unroll
    for (int i = 0; i < 16; ++i) red[i * 64 + lane] = acc[i];
  }
  WAIT_LGKM0();
  barrier_raw();
  char* ot = smem + 32768;
  if (br == 0) {
    const int m = m0 + orow;
    const float g = ((const float*)(ws + WS_G))[m];
    const float omg = rbf(1.0f - g);
#pragma unroll
    for (int gq = 0; gq < 4; ++gq) {
      float res[4];
#pragma unroll
      for (int e = 0; e < 4; ++e) {
        float a = rbf(acc[4 * gq + e]);
        float b = rbf(red[(4 * gq + e) * 64 + lane]);
        res[e] = rbf(g * a) + rbf(omg * b);
      }
      u32x2 v = {pk2(res[0], res[1]), pk2(res[2], res[3])};
      *(u32x2*)(ot + orow * 144 + (wn * 32 + 8 * gq + 4 * h) * 2) = v;
    }
  }
  WAIT_LGKM0();
  barrier_raw();
  {
    const int row = w * 8 + (lane >> 3), c = lane & 7;
    u32x4 v = *(const u32x4*)(ot + row * 144 + c * 16);
    *(u32x4*)(out + (size_t)(m0 + row) * D_ + n0 + c * 8) = v;
  }
}

extern "C" void kernel_launch(void* const* d_in, const int* in_sizes, int n_in, void* d_out, int out_size, void* d_ws,
                              size_t ws_size, hipStream_t stream) {
  const bf16_t* x = (const bf16_t*)d_in[0];
  const bf16_t* wq = (const bf16_t*)d_in[1];
  const bf16_t* wk = (const bf16_t*)d_in[2];
  const bf16_t* wv = (const bf16_t*)d_in[3];
  const bf16_t* woa = (const bf16_t*)d_in[4];
  const bf16_t* wqkv = (const bf16_t*)d_in[6];
  const bf16_t* wob = (const bf16_t*)d_in[7];
  const bf16_t* gw = (const bf16_t*)d_in[8];
  char* ws = (char*)d_ws;
  (void)hipFuncSetAttribute((const void*)k1_proj, hipFuncAttributeMaxDynamicSharedMemorySize, K1_LDS);
  (void)hipFuncSetAttribute((const void*)k2_attn, hipFuncAttributeMaxDynamicSharedMemorySize, K2_LDS);
  (void)hipFuncSetAttribute((const void*)k3_out, hipFuncAttributeMaxDynamicSharedMemorySize, K3_LDS);
  hipLaunchKernelGGL(k1_proj, dim3(256), dim3(512), K1_LDS, stream, x, wq, wk, wv, wqkv, gw, ws);
  hipLaunchKernelGGL(k2_attn, dim3(256), dim3(1024), K2_LDS, stream, ws);
  hipLaunchKernelGGL(k3_out, dim3(224), dim3(512), K3_LDS, stream, woa, wob, (const char*)ws, (bf16_t*)d_out);
}
```

```cpp
#include <hip/hip_runtime.h>
#include <hip/hip_bf16.h>
#include <stdint.h>

typedef __bf16 bf16_t;
typedef __bf16 bf16x8 __attribute__((ext_vector_type(8)));
typedef __bf16 bf16x4 __attribute__((ext_vector_type(4)));
typedef __bf16 bf16x2 __attribute__((ext_vector_type(2)));
typedef short s16x4 __attribute__((ext_vector_type(4)));
typedef float f32x16 __attribute__((ext_vector_type(16)));
typedef float f32x2 __attribute__((ext_vector_type(2)));
typedef float f32x4 __attribute__((ext_vector_type(4)));
typedef unsigned u32x2 __attribute__((ext_vector_type(2)));
typedef unsigned u32x4 __attribute__((ext_vector_type(4)));

#define LDS_AS __attribute__((address_space(3)))
#define GLB_AS __attribute__((address_space(1)))

constexpr int S_ = 2048, D_ = 448, H_ = 16, DH_ = 28;
constexpr int NQKV = 3 * D_;

constexpr int NP = 1792;
constexpr size_t WS_P = 0;
constexpr size_t WS_KF = WS_P + (size_t)S_ * NP * 2;
constexpr size_t WS_VF = WS_KF + (size_t)S_ * 32 * 2;
constexpr size_t WS_OAB = WS_VF + (size_t)S_ * 32 * 2;
constexpr size_t WS_G = WS_OAB + (size_t)S_ * 896 * 2;
constexpr size_t WS_KN = WS_G + (size_t)S_ * 4;
constexpr size_t WS_END = WS_KN + 256;

constexpr float SCALE_ = 0.1889822365046136f;
constexpr float CL2 = SCALE_ * 1.4426950408889634f;
constexpr float THR_L2 = 8.0f * 1.4426950408889634f;

__device__ __forceinline__ void glds16(const void* gsrc, unsigned lds_dst) {
  unsigned keep;
  asm volatile("s_mov_b32 %0, m0\n\ts_mov_b32 m0, %2\n\ts_nop 0\n\tglobal_load_lds_dwordx4 %1, off\n\ts_mov_b32 m0, %0"
               : "=&s"(keep) : "v"(gsrc), "s"(lds_dst) : "memory");
}
__device__ __forceinline__ void glds4(const void* gsrc, unsigned lds_dst) {
  unsigned keep;
  asm volatile("s_mov_b32 %0, m0\n\ts_mov_b32 m0, %2\n\ts_nop 0\n\tglobal_load_lds_dword %1, off\n\ts_mov_b32 m0, %0"
               : "=&s"(keep) : "v"(gsrc), "s"(lds_dst) : "memory");
}
__device__ __forceinline__ unsigned lds_off(const void* p) { return (unsigned)(size_t)(const LDS_AS char*)p; }
__device__ __forceinline__ void st16_wt(void* p, u32x4 v) {
  asm volatile("global_store_dwordx4 %0, %1, off sc1\n\ts_nop 1" ::"v"(p), "v"(v) : "memory");
}
__device__ __forceinline__ void st8_wt(void* p, u32x2 v) {
  asm volatile("global_store_dwordx2 %0, %1, off sc1\n\ts_nop 1" ::"v"(p), "v"(v) : "memory");
}
__device__ __forceinline__ void st4_wt(void* p, unsigned v) {
  asm volatile("global_store_dword %0, %1, off sc1\n\ts_nop 1" ::"v"(p), "v"(v) : "memory");
}
template <int CTRL> __device__ __forceinline__ float dpp_f(float x) {
  return __uint_as_float((unsigned)__builtin_amdgcn_update_dpp(0, (int)__float_as_uint(x), CTRL, 0xf, 0xf, true));
}
__device__ __forceinline__ float wave_max_nonneg(float v) {
  v = fmaxf(v, dpp_f<0xB1>(v));
  v = fmaxf(v, dpp_f<0x4E>(v));
  v = fmaxf(v, dpp_f<0x141>(v));
  v = fmaxf(v, dpp_f<0x140>(v));
  const float a = __uint_as_float(__builtin_amdgcn_readlane(__float_as_uint(v), 0));
  const float b = __uint_as_float(__builtin_amdgcn_readlane(__float_as_uint(v), 16));
  const float c = __uint_as_float(__builtin_amdgcn_readlane(__float_as_uint(v), 32));
  const float d = __uint_as_float(__builtin_amdgcn_readlane(__float_as_uint(v), 48));
  return fmaxf(fmaxf(a, b), fmaxf(c, d));
}
__device__ __forceinline__ float half_sum(float v) {
  auto rr = __builtin_amdgcn_permlane32_swap(__float_as_uint(v), __float_as_uint(v), false, false);
  return __uint_as_float(rr[0]) + __uint_as_float(rr[1]);
}
#define WAIT_VM(n) asm volatile("s_waitcnt vmcnt(" #n ")" ::: "memory")
#define WAIT_LGKM0() asm volatile("s_waitcnt lgkmcnt(0)" ::: "memory")
__device__ __forceinline__ void barrier_raw() {
  asm volatile("" ::: "memory");
  __builtin_amdgcn_s_barrier();
  asm volatile("" ::: "memory");
}
__device__ __forceinline__ bf16x8 lds_ld16(const char* p) { return *(const bf16x8*)p; }
__device__ __forceinline__ s16x4 lds_tr(const char* p) {
  return __builtin_amdgcn_ds_read_tr16_b64_v4i16((LDS_AS s16x4*)p);
}
__device__ __forceinline__ bf16x8 cat_tr(s16x4 a, s16x4 b) {
  typedef short s16x8 __attribute__((ext_vector_type(8)));
  s16x8 r = {a[0], a[1], a[2], a[3], b[0], b[1], b[2], b[3]};
  return __builtin_bit_cast(bf16x8, r);
}
__device__ __forceinline__ unsigned pk2(float a, float b) {
  f32x2 v = {a, b};
  bf16x2 r = __builtin_convertvector(v, bf16x2);
  return __builtin_bit_cast(unsigned, r);
}
__device__ __forceinline__ float rbf(float a) { return (float)(__bf16)a; }
__device__ __forceinline__ float bf2f(unsigned short u) { return __uint_as_float(((unsigned)u) << 16); }
__device__ __forceinline__ float bflo(unsigned u) { return __uint_as_float(u << 16); }
__device__ __forceinline__ float bfhi(unsigned u) { return __uint_as_float(u & 0xffff0000u); }
#define MFMA32(a, b, c) __builtin_amdgcn_mfma_f32_32x32x16_bf16((a), (b), (c), 0, 0, 0)

constexpr int K1_STAGE = 32768;
constexpr int K1_LDS = 4 * K1_STAGE;

__global__ __launch_bounds__(512) void k1_proj(const bf16_t* __restrict__ x, const bf16_t* __restrict__ wq,
                                               const bf16_t* __restrict__ wk, const bf16_t* __restrict__ wv,
                                               const bf16_t* __restrict__ wqkv, const bf16_t* __restrict__ gate_w,
                                               char* __restrict__ ws) {
  extern __shared__ __attribute__((aligned(16))) char smem[];
  const int tid = threadIdx.x, lane = tid & 63, w = __builtin_amdgcn_readfirstlane(tid >> 6);
  const int r = lane & 31, h = lane >> 5;
  const int bid = blockIdx.x;
  if (bid < 224) {
    const int xcd = bid & 7, idx = bid >> 3;
    const int mt = (xcd >> 1) * 4 + idx / 7, nt = (xcd & 1) * 7 + idx % 7;
    const int m0 = mt * 128, n0 = nt * 128;
    const bf16_t* xsrc[2];
    const bf16_t* wsrc[2];
#pragma unroll
    for (int i = 0; i < 2; ++i) {
      int row = (w * 2 + i) * 8 + (lane >> 3), slot = lane & 7;
      xsrc[i] = x + (size_t)(m0 + row) * D_ + ((slot ^ ((row >> 1) & 7)) * 8);
      int krow = (w * 2 + i) * 4 + (lane >> 4), wslot = lane & 15;
      int vcol = n0 + ((wslot ^ ((krow & 3) << 2)) * 8);
      if (n0 < 448 && vcol < 448) wsrc[i] = wq + (size_t)krow * 448 + vcol;
      else wsrc[i] = wqkv + (size_t)krow * NQKV + (vcol - 448);
    }
    int wstr[2];
#pragma unroll
    for (int i = 0; i < 2; ++i) {
      int krow = (w * 2 + i) * 4 + (lane >> 4), wslot = lane & 15;
      int vcol = n0 + ((wslot ^ ((krow & 3) << 2)) * 8);
      wstr[i] = (vcol < 448) ? 448 * 64 : NQKV * 64;
    }
    auto issue = [&](int s) {
      const unsigned base = lds_off(smem) + (s & 3) * K1_STAGE;
#pragma unroll
      for (int i = 0; i < 2; ++i) {
        glds16(xsrc[i] + s * 64, base + (w * 2 + i) * 1024);
        glds16(wsrc[i] + (size_t)s * wstr[i], base + 16384 + (w * 2 + i) * 1024);
      }
    };
    issue(0); issue(1); issue(2);
    const int wn = w & 3, wm = w >> 2;
    f32x16 acc0 = {}, acc1 = {};
    const int xrow0 = wm * 64 + r, xrow1 = wm * 64 + 32 + r;
    const int g16 = lane >> 4, q4 = (lane & 15) >> 2, p4 = lane & 3;
    const int trh = g16 >> 1, trb = g16 & 1;
    const int wchunk = wn * 4 + trb * 2 + (p4 >> 1);
#pragma unroll 1
    for (int s = 0; s < 7; ++s) {
      if (s < 5) WAIT_VM(8); else if (s == 5) WAIT_VM(4); else WAIT_VM(0);
      barrier_raw();
      if (s + 3 < 7) issue(s + 3);
      const char* xb = smem + (s & 3) * K1_STAGE;
      const char* wb = xb + 16384;
#pragma unroll
      for (int ks = 0; ks < 4; ++ks) {
        bf16x8 b0 = lds_ld16(xb + xrow0 * 128 + (((2 * ks + h) ^ ((xrow0 >> 1) & 7)) * 16));
        bf16x8 b1 = lds_ld16(xb + xrow1 * 128 + (((2 * ks + h) ^ ((xrow1 >> 1) & 7)) * 16));
        int k0 = ks * 16 + trh * 8 + q4;
        s16x4 a0 = lds_tr(wb + k0 * 256 + ((wchunk ^ ((k0 & 3) << 2)) * 16) + (p4 & 1) * 8);
        s16x4 a1 = lds_tr(wb + (k0 + 4) * 256 + ((wchunk ^ ((k0 & 3) << 2)) * 16) + (p4 & 1) * 8);
        bf16x8 a = cat_tr(a0, a1);
        acc0 = MFMA32(a, b0, acc0);
        acc1 = MFMA32(a, b1, acc1);
      }
    }
    barrier_raw();
#pragma unroll
    for (int mi = 0; mi < 2; ++mi) {
      const int ml = wm * 64 + mi * 32 + r;
      const f32x16& acc = mi ? acc1 : acc0;
      const float sc = (n0 + wn * 32 < 448) ? CL2 : 1.0f;
#pragma unroll
      for (int g = 0; g < 4; ++g) {
        u32x2 v = {pk2(acc[4 * g] * sc, acc[4 * g + 1] * sc), pk2(acc[4 * g + 2] * sc, acc[4 * g + 3] * sc)};
        *(u32x2*)(smem + ml * 272 + (wn * 32 + 8 * g + 4 * h) * 2) = v;
      }
    }
    WAIT_LGKM0();
    barrier_raw();
    bf16_t* P = (bf16_t*)(ws + WS_P);
#pragma unroll
    for (int i = 0; i < 4; ++i) {
      const int ml = w * 16 + i * 4 + (lane >> 4), c = lane & 15;
      u32x4 v = *(const u32x4*)(smem + ml * 272 + c * 16);
      st16_wt(P + (size_t)(m0 + ml) * NP + n0 + c * 8, v);
    }
  } else {
    const int jb = bid - 224, xcd = jb & 7;
    const int j = (xcd >> 1) * 8 + (xcd & 1) * 4 + (jb >> 3), m0 = j * 64;
    char* xl = smem;
    char* wl = smem + 64 * 896;
#pragma unroll
    for (int i = 0; i < 7; ++i) {
      int sl = (w * 7 + i) * 64 + lane;
      int row = sl / 56, slot = sl - row * 56;
      glds16(x + (size_t)(m0 + row) * D_ + ((slot ^ ((row >> 1) & 7)) * 8), lds_off(xl) + (w * 7 + i) * 1024);
    }
    {
      u32x2 wv_[14];
      const int pc = tid & 15;
      const bf16_t* wsrc = (pc < 8 ? wk : wv) + ((pc & 7) < 7 ? (pc & 7) * 4 : 0);
#pragma unroll
      for (int i = 0; i < 14; ++i) {
        int row = (tid >> 4) + i * 32;
        wv_[i] = *(const u32x2*)(wsrc + row * 28);
      }
#pragma unroll
      for (int i = 0; i < 14; ++i) {
        int row = (tid >> 4) + i * 32;
        u32x2 v = ((pc & 7) < 7) ? wv_[i] : (u32x2){0u, 0u};
        int chunk = pc >> 1;
        *(u32x2*)(wl + row * 128 + ((chunk ^ (((row >> 1) & 1) << 2)) * 16) + (pc & 1) * 8) = v;
      }
    }
    WAIT_VM(0);
    WAIT_LGKM0();
    barrier_raw();
    if (w < 4) {
      const int wn = w & 1, wm = w >> 1;
      const int xrow = wm * 32 + r;
      const int g16 = lane >> 4, q4 = (lane & 15) >> 2, p4 = lane & 3;
      const int trh = g16 >> 1, trb = g16 & 1;
      const int wchunk = wn * 4 + trb * 2 + (p4 >> 1);
      f32x16 acc = {};
#pragma unroll 4
      for (int ks = 0; ks < 28; ++ks) {
        bf16x8 b = lds_ld16(xl + xrow * 896 + (((2 * ks + h) ^ ((xrow >> 1) & 7)) * 16));
        int k0 = ks * 16 + trh * 8 + q4;
        s16x4 a0 = lds_tr(wl + k0 * 128 + ((wchunk ^ (((k0 >> 1) & 1) << 2)) * 16) + (p4 & 1) * 8);
        s16x4 a1 = lds_tr(wl + (k0 + 4) * 128 + ((wchunk ^ (((k0 >> 1) & 1) << 2)) * 16) + (p4 & 1) * 8);
        acc = MFMA32(cat_tr(a0, a1), b, acc);
      }
      const int key = m0 + wm * 32 + r;
      const int t = key >> 5;
      if (wn == 0) {
        bf16_t* kf = (bf16_t*)(ws + WS_KF);
        {
          float n2 = 0.f;
#pragma unroll
          for (int i = 0; i < 16; ++i) { const float kv = rbf(acc[i]); n2 = fmaf(kv, kv, n2); }
          n2 = wave_max_nonneg(half_sum(n2));
          if (lane == 0) st4_wt((float*)(ws + WS_KN) + t, __float_as_uint(n2));
        }
#pragma unroll
        for (int g = 0; g < 4; ++g) {
          const float e0 = (g == 3 && h == 1) ? 1.0f : acc[4 * g];
          u32x2 v = {pk2(e0, acc[4 * g + 1]), pk2(acc[4 * g + 2], acc[4 * g + 3])};
          st8_wt(kf + ((size_t)((t * 2 + (g >> 1)) * 64 + (g & 1) * 32 + r) * 8 + 4 * h), v);
        }
      } else {
        bf16_t* vt = (bf16_t*)(smem + 64 * 896 + 448 * 128) + wm * 1024;
        const int st = r >> 4, jj = ((r >> 3) & 1) * 4 + (r & 3), hh = (r >> 2) & 1;
#pragma unroll
        for (int g = 0; g < 4; ++g) {
#pragma unroll
          for (int e = 0; e < 4; ++e) {
            const int d = 8 * g + 4 * h + e;
            float val = acc[4 * g + e];
            if (d == 28) val = 1.0f;
            vt[(st * 64 + hh * 32 + d) * 8 + jj] = (__bf16)val;
          }
        }
        WAIT_LGKM0();
        bf16_t* vf = (bf16_t*)(ws + WS_VF) + (size_t)t * 1024;
#pragma unroll
        for (int i = 0; i < 2; ++i) st16_wt(vf + (i * 64 + lane) * 8, *(const u32x4*)(vt + (i * 64 + lane) * 8));
      }
    } else {
      const int t2 = tid - 256, row = t2 >> 2, qq = t2 & 3;
      const bf16_t* xr = x + (size_t)(m0 + row) * D_ + qq * 112;
      const bf16_t* gw = gate_w + qq * 112;
      float sum = 0.f;
#pragma unroll 2
      for (int c = 0; c < 14; ++c) {
        u32x4 xv = *(const u32x4*)(xr + c * 8);
        u32x4 gv = *(const u32x4*)(gw + c * 8);
#pragma unroll
        for (int e = 0; e < 4; ++e) {
          sum += rbf(bflo(xv[e]) * bflo(gv[e]));
          sum += rbf(bfhi(xv[e]) * bfhi(gv[e]));
        }
      }
      sum += __shfl_xor(sum, 1);
      sum += __shfl_xor(sum, 2);
      if (qq == 0) {
        float z = rbf(sum);
        float gsig = rbf(1.0f / (1.0f + __expf(-z)));
        st4_wt((float*)(ws + WS_G) + m0 + row, __float_as_uint(gsig));
      }
    }
  }
}

constexpr int K2_VOFF = 4 * 8192;
constexpr int K2_ROFF = 8 * 8192;
constexpr int K2_KVOFF = K2_ROFF + 8 * 1792;
constexpr int K2_POFF = K2_ROFF + 53 * 1024;
constexpr int K2_BOUT = K2_POFF + 2048;
constexpr int K2_LDS = K2_BOUT + 8 * 896;

__global__ __launch_bounds__(768) void k2_attn(char* __restrict__ ws) {
  extern __shared__ __attribute__((aligned(16))) char smem[];
  const int tid = threadIdx.x, lane = tid & 63, w = __builtin_amdgcn_readfirstlane(tid >> 6);
  const int r = lane & 31, h = lane >> 5;
  const int pos0 = blockIdx.x * 8;
  const int qg = w & 3, kh = (w >> 2) & 1;
  if (w >= 8) {
    const int li = w - 8;
    const bf16_t* P = (const bf16_t*)(ws + WS_P);
    const unsigned lbase = lds_off(smem);
    const bf16_t* kf = (const bf16_t*)(ws + WS_KF) + li * 512 + lane * 8;
    const bf16_t* vf = (const bf16_t*)(ws + WS_VF) + li * 512 + lane * 8;
    auto issueK = [&](int s) {
      glds16(kf + (size_t)(s * 4) * 512, lbase + (s & 3) * 8192 + li * 1024);
      glds16(kf + (size_t)(64 + s * 4) * 512, lbase + (s & 3) * 8192 + 4096 + li * 1024);
    };
    auto issueV = [&](int s) {
      glds16(vf + (size_t)(s * 4) * 512, lbase + K2_VOFF + (s & 3) * 8192 + li * 1024);
      glds16(vf + (size_t)(64 + s * 4) * 512, lbase + K2_VOFF + (s & 3) * 8192 + 4096 + li * 1024);
    };
    issueK(0);
#pragma unroll
    for (int i = 0; i < 4; ++i) {
      int p = li * 4 + i;
      p = p < 14 ? p : 13;
      const int o = p * 1024 + lane * 16, row = o / 1792, col = o - row * 1792;
      glds16(P + (size_t)(pos0 + row) * NP + (col >> 1), lbase + K2_ROFF + p * 1024);
    }
    glds4((const float*)(ws + WS_KN) + lane, lbase + K2_POFF);
    issueK(1); issueV(0);
    WAIT_VM(4);
    barrier_raw();
    issueK(2); issueV(1); issueK(3); issueV(2);
#pragma unroll 1
    for (int s = 0; s < 16; ++s) {
      if (s < 13) WAIT_VM(8); else if (s == 13) WAIT_VM(4); else WAIT_VM(0);
      barrier_raw();
      if (s < 3) {
#pragma unroll 1
        for (int i = (s ? 1 + 3 * s : 0); i < 4 + 3 * s; ++i) {
          int p = 14 + li * 10 + i;
          p = p > 52 ? 52 : p;
          int o = p * 1024 + lane * 16;
          o = o < 53760 ? o : 53760 - 16;
          const int row = o / 1792, col = o - row * 1792;
          int prow = pos0 - 22 + row;
          prow = prow < 0 ? 0 : prow;
          glds16(P + (size_t)prow * NP + 896 + (col >> 1), lbase + K2_ROFF + p * 1024);
        }
      }
      if (s < 12) issueK(s + 4);
      if (s < 13) issueV(s + 3);
    }
    barrier_raw(); barrier_raw(); barrier_raw();
    return;
  }
  barrier_raw();
  bf16x8 q0, q1;
  {
    const int qi = qg * 32 + r;
    const char* qp = smem + K2_ROFF + (qi & 7) * 1792 + (qi >> 3) * 56 + 16 * h;
    u32x2 a = *(const u32x2*)qp, b = *(const u32x2*)(qp + 8), c = *(const u32x2*)(qp + 32), d = *(const u32x2*)(qp + (h ? 32 : 40));
    if (h) d = (u32x2){0u, 0u};
    u32x4 t0 = {a[0], a[1], b[0], b[1]}, t1 = {c[0], c[1], d[0], d[1]};
    q0 = __builtin_bit_cast(bf16x8, t0); q1 = __builtin_bit_cast(bf16x8, t1);
  }
  f32x16 sa0, sa1, sb0, sb1;
  {
    const char* kb = smem + kh * 4096 + lane * 16;
    bf16x8 k00 = lds_ld16(kb), k01 = lds_ld16(kb + 1024), k10 = lds_ld16(kb + 2048), k11 = lds_ld16(kb + 3072);
    sa0 = MFMA32(k00, q0, (f32x16){}); sa1 = MFMA32(k10, q0, (f32x16){});
    sa0 = MFMA32(k01, q1, sa0); sa1 = MFMA32(k11, q1, sa1);
  }
  float sbound;
  {
    float n2 = 0.f;
#pragma unroll
    for (int i = 0; i < 8; ++i) { n2 = fmaf((float)q0[i], (float)q0[i], n2); n2 = fmaf((float)q1[i], (float)q1[i], n2); }
    n2 = half_sum(n2);
    const float kn2 = wave_max_nonneg(((const float*)(smem + K2_POFF))[lane]);
    sbound = sqrtf(n2 * kn2) * 1.01f + 1.0f;
  }
  bool fast = false;
  f32x16 o = {};
  float mref = 0.f;
  auto iter = [&](f32x16& c0, f32x16& c1, f32x16& n0, f32x16& n1, const int s) __attribute__((always_inline)) {
    barrier_raw();
    const char* kb = smem + ((s + 1) & 3) * 8192 + kh * 4096 + lane * 16;
    const char* vb = smem + K2_VOFF + (s & 3) * 8192 + kh * 4096 + lane * 16;
    bf16x8 k00 = lds_ld16(kb), k01 = lds_ld16(kb + 1024), k10 = lds_ld16(kb + 2048), k11 = lds_ld16(kb + 3072);
    bf16x8 v00 = lds_ld16(vb), v01 = lds_ld16(vb + 1024), v10 = lds_ld16(vb + 2048), v11 = lds_ld16(vb + 3072);
    __builtin_amdgcn_sched_barrier(0);
    if (!fast) {
      float pmax = -3.0e38f;
#pragma unroll
      for (int i = 0; i < 16; i += 2) pmax = fmaxf(fmaxf(pmax, c0[i]), c0[i + 1]);
#pragma unroll
      for (int i = 0; i < 16; i += 2) pmax = fmaxf(fmaxf(pmax, c1[i]), c1[i + 1]);
      {
        auto rr = __builtin_amdgcn_permlane32_swap(__float_as_uint(pmax), __float_as_uint(pmax), false, false);
        pmax = fmaxf(fmaxf(pmax, __uint_as_float(rr[0])), __uint_as_float(rr[1]));
      }
      if (s == 0 || !__all(pmax <= THR_L2)) {
        const float mnew = rbf(mref + (s == 0 ? pmax : fmaxf(pmax, 0.f)));
        const float delta = mnew - mref;
        mref = mnew;
        if (s != 0) {
          const float alpha = __builtin_amdgcn_exp2f(-delta);
#pragma unroll
          for (int i = 0; i < 16; ++i) o[i] *= alpha;
        }
#pragma unroll
        for (int i = 0; i < 16; ++i) { c0[i] -= delta; c1[i] -= delta; }
        if (h) q1[4] = (__bf16)(-mnew);
      }
      if (s == 0) fast = __all(sbound - mref <= 64.0f);
    }
#pragma unroll
    for (int i = 0; i < 8; ++i) c0[i] = __builtin_amdgcn_exp2f(c0[i]);
    __builtin_amdgcn_sched_barrier(0);
    n0 = MFMA32(k00, q0, (f32x16){}); n1 = MFMA32(k10, q0, (f32x16){});
#pragma unroll
    for (int i = 8; i < 16; ++i) c0[i] = __builtin_amdgcn_exp2f(c0[i]);
    u32x4 p00 = {pk2(c0[0], c0[1]), pk2(c0[2], c0[3]), pk2(c0[4], c0[5]), pk2(c0[6], c0[7])};
    u32x4 p01 = {pk2(c0[8], c0[9]), pk2(c0[10], c0[11]), pk2(c0[12], c0[13]), pk2(c0[14], c0[15])};
    __builtin_amdgcn_sched_barrier(0);
    n0 = MFMA32(k01, q1, n0); n1 = MFMA32(k11, q1, n1);
#pragma unroll
    for (int i = 0; i < 8; ++i) c1[i] = __builtin_amdgcn_exp2f(c1[i]);
    __builtin_amdgcn_sched_barrier(0);
    o = MFMA32(v00, __builtin_bit_cast(bf16x8, p00), o);
    o = MFMA32(v01, __builtin_bit_cast(bf16x8, p01), o);
#pragma unroll
    for (int i = 8; i < 16; ++i) c1[i] = __builtin_amdgcn_exp2f(c1[i]);
    u32x4 p10 = {pk2(c1[0], c1[1]), pk2(c1[2], c1[3]), pk2(c1[4], c1[5]), pk2(c1[6], c1[7])};
    u32x4 p11 = {pk2(c1[8], c1[9]), pk2(c1[10], c1[11]), pk2(c1[12], c1[13]), pk2(c1[14], c1[15])};
    __builtin_amdgcn_sched_barrier(0);
    o = MFMA32(v10, __builtin_bit_cast(bf16x8, p10), o);
    o = MFMA32(v11, __builtin_bit_cast(bf16x8, p11), o);
  };
#pragma unroll 1
  for (int s = 0; s < 16; s += 2) {
    iter(sa0, sa1, sb0, sb1, s);
    iter(sb0, sb1, sa0, sa1, s + 1);
  }
  barrier_raw();
  char* outt = smem + 40960;
  {
    char* red = smem + (qg * 2 + kh) * 5120 + lane * 16;
#pragma unroll
    for (int g = 0; g < 4; ++g) *(f32x4*)(red + g * 1024) = (f32x4){o[4 * g], o[4 * g + 1], o[4 * g + 2], o[4 * g + 3]};
    *(float*)(smem + (qg * 2 + kh) * 5120 + 4096 + lane * 4) = mref;
  }
  {
    const int pl = w, hd = lane >> 2, sub = lane & 3;
    const char* qb = smem + K2_ROFF + pl * 1792 + 896 + hd * 56;
    const char* kb = smem + K2_KVOFF + (14 + pl - 2 * sub) * 1792 + hd * 56;
    float acc0 = 0.f, acc1 = 0.f;
#pragma unroll
    for (int i = 0; i < 7; ++i) {
      const u32x2 qv = *(const u32x2*)(qb + i * 8), ka = *(const u32x2*)(kb + i * 8), kc = *(const u32x2*)(kb - 8 * 1792 + i * 8);
      const float q0f = bflo(qv[0]), q1f = bfhi(qv[0]), q2f = bflo(qv[1]), q3f = bfhi(qv[1]);
      acc0 = fmaf(q0f, bflo(ka[0]), acc0); acc0 = fmaf(q1f, bfhi(ka[0]), acc0);
      acc0 = fmaf(q2f, bflo(ka[1]), acc0); acc0 = fmaf(q3f, bfhi(ka[1]), acc0);
      acc1 = fmaf(q0f, bflo(kc[0]), acc1); acc1 = fmaf(q1f, bfhi(kc[0]), acc1);
      acc1 = fmaf(q2f, bflo(kc[1]), acc1); acc1 = fmaf(q3f, bfhi(kc[1]), acc1);
    }
    float lv0 = rbf(rbf(acc0) * SCALE_), lv1 = rbf(rbf(acc1) * SCALE_);
    if (pos0 + pl - 2 * sub < 0) lv0 = rbf(-1e9f);
    if (pos0 + pl - 2 * sub - 8 < 0) lv1 = rbf(-1e9f);
    float mx = fmaxf(lv0, lv1);
    mx = fmaxf(mx, dpp_f<0xB1>(mx));
    mx = fmaxf(mx, dpp_f<0x4E>(mx));
    const float e0 = __expf(lv0 - mx), e1 = __expf(lv1 - mx);
    float sum = e0 + e1;
    sum += dpp_f<0xB1>(sum);
    sum += dpp_f<0x4E>(sum);
    const float inv = 1.0f / sum;
    const float p0 = rbf(e0 * inv), p1 = rbf(e1 * inv);
    float pw[8];
    pw[0] = dpp_f<0x00>(p0); pw[1] = dpp_f<0x55>(p0); pw[2] = dpp_f<0xAA>(p0); pw[3] = dpp_f<0xFF>(p0);
    pw[4] = dpp_f<0x00>(p1); pw[5] = dpp_f<0x55>(p1); pw[6] = dpp_f<0xAA>(p1); pw[7] = dpp_f<0xFF>(p1);
    const int ca = hd * 56 + sub * 8, cb = hd * 56 + (sub < 3 ? sub + 4 : sub) * 8;
    float oa[4] = {0.f, 0.f, 0.f, 0.f}, ob[4] = {0.f, 0.f, 0.f, 0.f};
#pragma unroll
    for (int ww = 0; ww < 8; ++ww) {
      const char* vr = smem + K2_KVOFF + (14 + pl - 2 * ww) * 1792 + 896;
      const u32x2 va = *(const u32x2*)(vr + ca), vb = *(const u32x2*)(vr + cb);
      oa[0] = fmaf(pw[ww], bflo(va[0]), oa[0]); oa[1] = fmaf(pw[ww], bfhi(va[0]), oa[1]);
      oa[2] = fmaf(pw[ww], bflo(va[1]), oa[2]); oa[3] = fmaf(pw[ww], bfhi(va[1]), oa[3]);
      ob[0] = fmaf(pw[ww], bflo(vb[0]), ob[0]); ob[1] = fmaf(pw[ww], bfhi(vb[0]), ob[1]);
      ob[2] = fmaf(pw[ww], bflo(vb[1]), ob[2]); ob[3] = fmaf(pw[ww], bfhi(vb[1]), ob[3]);
    }
    *(u32x2*)(smem + K2_BOUT + pl * 896 + ca) = (u32x2){pk2(oa[0], oa[1]), pk2(oa[2], oa[3])};
    if (sub < 3) *(u32x2*)(smem + K2_BOUT + pl * 896 + cb) = (u32x2){pk2(ob[0], ob[1]), pk2(ob[2], ob[3])};
  }
  WAIT_LGKM0();
  barrier_raw();
  {
    const char* base = smem + qg * 2 * 5120;
    const float m0_ = *(const float*)(base + 4096 + lane * 4), m1_ = *(const float*)(base + 5120 + 4096 + lane * 4);
    const float l0 = *(const float*)(base + 3 * 1024 + (32 + r) * 16), l1 = *(const float*)(base + 5120 + 3 * 1024 + (32 + r) * 16);
    const float mn = fmaxf(m0_, m1_);
    float a0 = __builtin_amdgcn_exp2f(m0_ - mn), a1 = __builtin_amdgcn_exp2f(m1_ - mn);
    const float inv = 1.0f / (l0 * a0 + l1 * a1);
    a0 *= inv; a1 *= inv;
    const int qi = qg * 32 + r;
#pragma unroll
    for (int gg = 0; gg < 2; ++gg) {
      const int g = 2 * kh + gg;
      const f32x4 acc = *(const f32x4*)(base + g * 1024 + lane * 16) * a0 + *(const f32x4*)(base + 5120 + g * 1024 + lane * 16) * a1;
      if (!(g == 3 && h == 1))
        *(u32x2*)(outt + (qi & 7) * 896 + (qi >> 3) * 56 + 8 * h + 16 * g) = (u32x2){pk2(acc[0], acc[1]), pk2(acc[2], acc[3])};
    }
  }
  WAIT_LGKM0();
  barrier_raw();
  if (lane < 56) {
    char* dst = ws + WS_OAB + (size_t)(pos0 + w) * 1792 + lane * 16;
    st16_wt(dst, *(const u32x4*)(outt + w * 896 + lane * 16));
    st16_wt(dst + 896, *(const u32x4*)(smem + K2_BOUT + w * 896 + lane * 16));
  }
}

constexpr int K3_STAGE = 32768;
constexpr int K3_LDS = 4 * K3_STAGE;

__global__ __launch_bounds__(512) void k3_out(const bf16_t* __restrict__ woa, const bf16_t* __restrict__ wob,
                                              const char* __restrict__ ws, bf16_t* __restrict__ out) {
  extern __shared__ __attribute__((aligned(16))) char smem[];
  const int tid = threadIdx.x, lane = tid & 63, w = __builtin_amdgcn_readfirstlane(tid >> 6);
  const int r = lane & 31, h = lane >> 5;
  const int bid = blockIdx.x;
  const int xcd = bid & 7, idx = bid >> 3;
  const int mt = xcd * 4 + idx / 7, nt = idx % 7;
  const int m0 = mt * 64, n0 = nt * 64;
  const int br = w >> 2, wl = w & 3;
  const bf16_t* oab = (const bf16_t*)(ws + WS_OAB);
  const bf16_t* wo = br ? wob : woa;
  const bf16_t* osrc[2];
  const bf16_t* wsrc[2];
#pragma unroll
  for (int i = 0; i < 2; ++i) {
    int row = (wl * 2 + i) * 8 + (lane >> 3), slot = lane & 7;
    osrc[i] = oab + (size_t)(m0 + row) * 896 + br * 448 + ((slot ^ ((row >> 1) & 7)) * 8);
    wsrc[i] = wo + (size_t)row * 448 + n0 + ((slot ^ (((row >> 1) & 1) << 2)) * 8);
  }
  auto issue = [&](int s) {
    const unsigned base = lds_off(smem) + (s & 3) * K3_STAGE + br * 16384;
#pragma unroll
    for (int i = 0; i < 2; ++i) {
      glds16(osrc[i] + s * 64, base + (wl * 2 + i) * 1024);
      glds16(wsrc[i] + (size_t)s * 64 * 448, base + 8192 + (wl * 2 + i) * 1024);
    }
  };
  issue(0); issue(1); issue(2);
  const int wn = wl & 1, wm = wl >> 1;
  const int orow = wm * 32 + r;
  const int g16 = lane >> 4, q4 = (lane & 15) >> 2, p4 = lane & 3;
  const int trh = g16 >> 1, trb = g16 & 1;
  const int wchunk = wn * 4 + trb * 2 + (p4 >> 1);
  f32x16 acc = {};
#pragma unroll 1
  for (int s = 0; s < 7; ++s) {
    if (s < 5) WAIT_VM(8); else if (s == 5) WAIT_VM(4); else WAIT_VM(0);
    barrier_raw();
    if (s + 3 < 7) issue(s + 3);
    const char* ob = smem + (s & 3) * K3_STAGE + br * 16384;
    const char* wb = ob + 8192;
#pragma unroll
    for (int ks = 0; ks < 4; ++ks) {
      bf16x8 b = lds_ld16(ob + orow * 128 + (((2 * ks + h) ^ ((orow >> 1) & 7)) * 16));
      int k0 = ks * 16 + trh * 8 + q4;
      s16x4 a0 = lds_tr(wb + k0 * 128 + ((wchunk ^ (((k0 >> 1) & 1) << 2)) * 16) + (p4 & 1) * 8);
      s16x4 a1 = lds_tr(wb + (k0 + 4) * 128 + ((wchunk ^ (((k0 >> 1) & 1) << 2)) * 16) + (p4 & 1) * 8);
      acc = MFMA32(cat_tr(a0, a1), b, acc);
    }
  }
  barrier_raw();
  float* red = (float*)smem + wl * (16 * 64);
  if (br == 1) {
#pragma unroll
    for (int i = 0; i < 16; ++i) red[i * 64 + lane] = acc[i];
  }
  WAIT_LGKM0();
  barrier_raw();
  char* ot = smem + 32768;
  if (br == 0) {
    const int m = m0 + orow;
    const float g = ((const float*)(ws + WS_G))[m];
    const float omg = rbf(1.0f - g);
#pragma unroll
    for (int gq = 0; gq < 4; ++gq) {
      float res[4];
#pragma unroll
      for (int e = 0; e < 4; ++e) {
        float a = rbf(acc[4 * gq + e]);
        float b = rbf(red[(4 * gq + e) * 64 + lane]);
        res[e] = rbf(g * a) + rbf(omg * b);
      }
      u32x2 v = {pk2(res[0], res[1]), pk2(res[2], res[3])};
      *(u32x2*)(ot + orow * 144 + (wn * 32 + 8 * gq + 4 * h) * 2) = v;
    }
  }
  WAIT_LGKM0();
  barrier_raw();
  {
    const int row = w * 8 + (lane >> 3), c = lane & 7;
    u32x4 v = *(const u32x4*)(ot + row * 144 + c * 16);
    st16_wt(out + (size_t)(m0 + row) * D_ + n0 + c * 8, v);
  }
}

extern "C" void kernel_launch(void* const* d_in, const int* in_sizes, int n_in, void* d_out, int out_size, void* d_ws,
                              size_t ws_size, hipStream_t stream) {
  const bf16_t* x = (const bf16_t*)d_in[0];
  const bf16_t* wq = (const bf16_t*)d_in[1];
  const bf16_t* wk = (const bf16_t*)d_in[2];
  const bf16_t* wv = (const bf16_t*)d_in[3];
  const bf16_t* woa = (const bf16_t*)d_in[4];
  const bf16_t* wqkv = (const bf16_t*)d_in[6];
  const bf16_t* wob = (const bf16_t*)d_in[7];
  const bf16_t* gw = (const bf16_t*)d_in[8];
  char* ws = (char*)d_ws;
  (void)hipFuncSetAttribute((const void*)k1_proj, hipFuncAttributeMaxDynamicSharedMemorySize, K1_LDS);
  (void)hipFuncSetAttribute((const void*)k2_attn, hipFuncAttributeMaxDynamicSharedMemorySize, K2_LDS);
  (void)hipFuncSetAttribute((const void*)k3_out, hipFuncAttributeMaxDynamicSharedMemorySize, K3_LDS);
  hipLaunchKernelGGL(k1_proj, dim3(256), dim3(512), K1_LDS, stream, x, wq, wk, wv, wqkv, gw, ws);
  hipLaunchKernelGGL(k2_attn, dim3(256), dim3(768), K2_LDS, stream, ws);
  hipLaunchKernelGGL(k3_out, dim3(224), dim3(512), K3_LDS, stream, woa, wob, (const char*)ws, (bf16_t*)d_out);
}
```

```cpp
#include <hip/hip_runtime.h>
#include <hip/hip_bf16.h>
#include <stdint.h>

typedef __bf16 bf16_t;
typedef __bf16 bf16x8 __attribute__((ext_vector_type(8)));
typedef __bf16 bf16x4 __attribute__((ext_vector_type(4)));
typedef __bf16 bf16x2 __attribute__((ext_vector_type(2)));
typedef short s16x4 __attribute__((ext_vector_type(4)));
typedef float f32x16 __attribute__((ext_vector_type(16)));
typedef float f32x2 __attribute__((ext_vector_type(2)));
typedef float f32x4 __attribute__((ext_vector_type(4)));
typedef unsigned u32x2 __attribute__((ext_vector_type(2)));
typedef unsigned u32x4 __attribute__((ext_vector_type(4)));

#define LDS_AS __attribute__((address_space(3)))
#define GLB_AS __attribute__((address_space(1)))

constexpr int S_ = 2048, D_ = 448, H_ = 16, DH_ = 28;
constexpr int NQKV = 3 * D_;

constexpr int NP = 1792;
constexpr size_t WS_P = 0;
constexpr size_t WS_KF = WS_P + (size_t)S_ * NP * 2;
constexpr size_t WS_VF = WS_KF + (size_t)S_ * 32 * 2;
constexpr size_t WS_OAB = WS_VF + (size_t)S_ * 32 * 2;
constexpr size_t WS_G = WS_OAB + (size_t)S_ * 896 * 2;
constexpr size_t WS_KN = WS_G + (size_t)S_ * 4;
constexpr size_t WS_END = WS_KN + 256;

constexpr float SCALE_ = 0.1889822365046136f;
constexpr float CL2 = SCALE_ * 1.4426950408889634f;
constexpr float THR_L2 = 8.0f * 1.4426950408889634f;

__device__ __forceinline__ void glds16(const void* gsrc, unsigned lds_dst) {
  unsigned keep;
  asm volatile("s_mov_b32 %0, m0\n\ts_mov_b32 m0, %2\n\ts_nop 0\n\tglobal_load_lds_dwordx4 %1, off\n\ts_mov_b32 m0, %0"
               : "=&s"(keep) : "v"(gsrc), "s"(lds_dst) : "memory");
}
__device__ __forceinline__ void glds4(const void* gsrc, unsigned lds_dst) {
  unsigned keep;
  asm volatile("s_mov_b32 %0, m0\n\ts_mov_b32 m0, %2\n\ts_nop 0\n\tglobal_load_lds_dword %1, off\n\ts_mov_b32 m0, %0"
               : "=&s"(keep) : "v"(gsrc), "s"(lds_dst) : "memory");
}
__device__ __forceinline__ unsigned lds_off(const void* p) { return (unsigned)(size_t)(const LDS_AS char*)p; }
__device__ __forceinline__ void st16_wt(void* p, u32x4 v) {
  asm volatile("global_store_dwordx4 %0, %1, off sc1\n\ts_nop 1" ::"v"(p), "v"(v) : "memory");
}
__device__ __forceinline__ void st8_wt(void* p, u32x2 v) {
  asm volatile("global_store_dwordx2 %0, %1, off sc1\n\ts_nop 1" ::"v"(p), "v"(v) : "memory");
}
__device__ __forceinline__ void st4_wt(void* p, unsigned v) {
  asm volatile("global_store_dword %0, %1, off sc1\n\ts_nop 1" ::"v"(p), "v"(v) : "memory");
}
template <int CTRL> __device__ __forceinline__ float dpp_f(float x) {
  return __uint_as_float((unsigned)__builtin_amdgcn_update_dpp(0, (int)__float_as_uint(x), CTRL, 0xf, 0xf, true));
}
__device__ __forceinline__ float wave_max_nonneg(float v) {
  v = fmaxf(v, dpp_f<0xB1>(v));
  v = fmaxf(v, dpp_f<0x4E>(v));
  v = fmaxf(v, dpp_f<0x141>(v));
  v = fmaxf(v, dpp_f<0x140>(v));
  const float a = __uint_as_float(__builtin_amdgcn_readlane(__float_as_uint(v), 0));
  const float b = __uint_as_float(__builtin_amdgcn_readlane(__float_as_uint(v), 16));
  const float c = __uint_as_float(__builtin_amdgcn_readlane(__float_as_uint(v), 32));
  const float d = __uint_as_float(__builtin_amdgcn_readlane(__float_as_uint(v), 48));
  return fmaxf(fmaxf(a, b), fmaxf(c, d));
}
__device__ __forceinline__ float half_sum(float v) {
  auto rr = __builtin_amdgcn_permlane32_swap(__float_as_uint(v), __float_as_uint(v), false, false);
  return __uint_as_float(rr[0]) + __uint_as_float(rr[1]);
}
#define WAIT_VM(n) asm volatile("s_waitcnt vmcnt(" #n ")" ::: "memory")
#define WAIT_LGKM0() asm volatile("s_waitcnt lgkmcnt(0)" ::: "memory")
__device__ __forceinline__ void barrier_raw() {
  asm volatile("" ::: "memory");
  __builtin_amdgcn_s_barrier();
  asm volatile("" ::: "memory");
}
__device__ __forceinline__ bf16x8 lds_ld16(const char* p) { return *(const bf16x8*)p; }
__device__ __forceinline__ s16x4 lds_tr(const char* p) {
  return __builtin_amdgcn_ds_read_tr16_b64_v4i16((LDS_AS s16x4*)p);
}
__device__ __forceinline__ bf16x8 cat_tr(s16x4 a, s16x4 b) {
  typedef short s16x8 __attribute__((ext_vector_type(8)));
  s16x8 r = {a[0], a[1], a[2], a[3], b[0], b[1], b[2], b[3]};
  return __builtin_bit_cast(bf16x8, r);
}
__device__ __forceinline__ unsigned pk2(float a, float b) {
  f32x2 v = {a, b};
  bf16x2 r = __builtin_convertvector(v, bf16x2);
  return __builtin_bit_cast(unsigned, r);
}
__device__ __forceinline__ float rbf(float a) { return (float)(__bf16)a; }
__device__ __forceinline__ float bf2f(unsigned short u) { return __uint_as_float(((unsigned)u) << 16); }
__device__ __forceinline__ float bflo(unsigned u) { return __uint_as_float(u << 16); }
__device__ __forceinline__ float bfhi(unsigned u) { return __uint_as_float(u & 0xffff0000u); }
#define MFMA32(a, b, c) __builtin_amdgcn_mfma_f32_32x32x16_bf16((a), (b), (c), 0, 0, 0)

constexpr int K1_WOFF = 7 * 16384;
constexpr int K1_SOFF = K1_WOFF + 3 * 8192;
constexpr int K1_LDS = K1_SOFF + 128 * 144;

__global__ __launch_bounds__(512) void k1_proj(const bf16_t* __restrict__ x, const bf16_t* __restrict__ wq,
                                               const bf16_t* __restrict__ wk, const bf16_t* __restrict__ wv,
                                               const bf16_t* __restrict__ wqkv, const bf16_t* __restrict__ gate_w,
                                               char* __restrict__ ws) {
  extern __shared__ __attribute__((aligned(16))) char smem[];
  const int tid = threadIdx.x, lane = tid & 63, w = __builtin_amdgcn_readfirstlane(tid >> 6);
  const int r = lane & 31, h = lane >> 5;
  const int bid = blockIdx.x;
  if (bid < 224) {
    const int xcd = bid & 7, idx = bid >> 3;
    const int mt = (xcd >> 1) * 4 + idx / 7, nt = (xcd & 1) * 7 + idx % 7;
    const int m0 = mt * 128, n0 = nt * 128;
    const unsigned lb = lds_off(smem);
    const bf16_t* xsrc[2];
#pragma unroll
    for (int i = 0; i < 2; ++i) {
      const int row = (w * 2 + i) * 8 + (lane >> 3), slot = lane & 7;
      xsrc[i] = x + (size_t)(m0 + row) * D_ + ((slot ^ ((row >> 1) & 7)) * 8);
    }
    const bf16_t* wsrc[2];
    int wstr[2];
#pragma unroll
    for (int hf = 0; hf < 2; ++hf) {
      const int krow = w * 8 + (lane >> 3), slot = lane & 7;
      const int vc = n0 + hf * 64;
      const int cc = (slot ^ (((krow >> 1) & 1) << 2)) * 8;
      wstr[hf] = vc < 448 ? 448 : NQKV;
      wsrc[hf] = (vc < 448 ? wq + vc : wqkv + (vc - 448)) + (size_t)krow * wstr[hf] + cc;
    }
    auto issueX = [&](int c) {
      glds16(xsrc[0] + c * 64, lb + c * 16384 + (w * 2) * 1024);
      glds16(xsrc[1] + c * 64, lb + c * 16384 + (w * 2 + 1) * 1024);
    };
    auto issueW = [&](int t) {
      const int hf = t / 7, c = t % 7;
      glds16(wsrc[hf] + (size_t)c * 64 * wstr[hf], lb + K1_WOFF + (t % 3) * 8192 + w * 1024);
    };
    issueX(0); issueW(0); issueX(1); issueW(1); issueX(2);
    const int wn = w & 1, wm = w >> 1;
    const int xrow = wm * 32 + r;
    const int g16 = lane >> 4, q4 = (lane & 15) >> 2, p4 = lane & 3;
    const int trh = g16 >> 1, trb = g16 & 1;
    const int wchunk = wn * 4 + trb * 2 + (p4 >> 1);
    bf16_t* P = (bf16_t*)(ws + WS_P);
    char* stg = smem + K1_SOFF;
    auto stage_out = [&](const f32x16& acc, int hf) {
      const float sc = (n0 + hf * 64 + wn * 32 < 448) ? CL2 : 1.0f;
#pragma unroll
      for (int g = 0; g < 4; ++g) {
        u32x2 v = {pk2(acc[4 * g] * sc, acc[4 * g + 1] * sc), pk2(acc[4 * g + 2] * sc, acc[4 * g + 3] * sc)};
        *(u32x2*)(stg + xrow * 144 + (wn * 32 + 8 * g + 4 * h) * 2) = v;
      }
      WAIT_LGKM0();
    };
    auto store_out = [&](int hf) {
#pragma unroll
      for (int i = 0; i < 2; ++i) {
        const int ml = (w * 2 + i) * 8 + (lane >> 3), c = lane & 7;
        st16_wt(P + (size_t)(m0 + ml) * NP + n0 + hf * 64 + c * 8, *(const u32x4*)(stg + ml * 144 + c * 16));
      }
    };
    f32x16 acc = {};
#pragma unroll
    for (int t = 0; t < 14; ++t) {
      if (t < 5) WAIT_VM(5);
      else if (t == 5 || t == 8 || t == 9) WAIT_VM(3);
      else if (t < 13) WAIT_VM(1);
      else WAIT_VM(0);
      barrier_raw();
      if (t < 12) issueW(t + 2);
      if (t < 4) issueX(t + 3);
      if (t == 7) store_out(0);
      const char* xb = smem + (t % 7) * 16384;
      const char* wb = smem + K1_WOFF + (t % 3) * 8192;
#pragma unroll
      for (int ks = 0; ks < 4; ++ks) {
        bf16x8 b = lds_ld16(xb + xrow * 128 + (((2 * ks + h) ^ ((xrow >> 1) & 7)) * 16));
        const int k0 = ks * 16 + trh * 8 + q4;
        s16x4 a0 = lds_tr(wb + k0 * 128 + ((wchunk ^ (((k0 >> 1) & 1) << 2)) * 16) + (p4 & 1) * 8);
        s16x4 a1 = lds_tr(wb + (k0 + 4) * 128 + ((wchunk ^ (((k0 >> 1) & 1) << 2)) * 16) + (p4 & 1) * 8);
        acc = MFMA32(cat_tr(a0, a1), b, acc);
      }
      if (t == 6) { stage_out(acc, 0); acc = (f32x16){}; }
    }
    barrier_raw();
    stage_out(acc, 1);
    barrier_raw();
    store_out(1);
  } else {
    const int jb = bid - 224, xcd = jb & 7;
    const int j = (xcd >> 1) * 8 + (xcd & 1) * 4 + (jb >> 3), m0 = j * 64;
    char* xl = smem;
    char* wl = smem + 64 * 896;
#pragma unroll
    for (int i = 0; i < 7; ++i) {
      int sl = (w * 7 + i) * 64 + lane;
      int row = sl / 56, slot = sl - row * 56;
      glds16(x + (size_t)(m0 + row) * D_ + ((slot ^ ((row >> 1) & 7)) * 8), lds_off(xl) + (w * 7 + i) * 1024);
    }
    {
      u32x2 wv_[14];
      const int pc = tid & 15;
      const bf16_t* wsrc = (pc < 8 ? wk : wv) + ((pc & 7) < 7 ? (pc & 7) * 4 : 0);
#pragma unroll
      for (int i = 0; i < 14; ++i) {
        int row = (tid >> 4) + i * 32;
        wv_[i] = *(const u32x2*)(wsrc + row * 28);
      }
#pragma unroll
      for (int i = 0; i < 14; ++i) {
        int row = (tid >> 4) + i * 32;
        u32x2 v = ((pc & 7) < 7) ? wv_[i] : (u32x2){0u, 0u};
        int chunk = pc >> 1;
        *(u32x2*)(wl + row * 128 + ((chunk ^ (((row >> 1) & 1) << 2)) * 16) + (pc & 1) * 8) = v;
      }
    }
    WAIT_VM(0);
    WAIT_LGKM0();
    barrier_raw();
    if (w < 4) {
      const int wn = w & 1, wm = w >> 1;
      const int xrow = wm * 32 + r;
      const int g16 = lane >> 4, q4 = (lane & 15) >> 2, p4 = lane & 3;
      const int trh = g16 >> 1, trb = g16 & 1;
      const int wchunk = wn * 4 + trb * 2 + (p4 >> 1);
      f32x16 acc = {};
#pragma unroll 4
      for (int ks = 0; ks < 28; ++ks) {
        bf16x8 b = lds_ld16(xl + xrow * 896 + (((2 * ks + h) ^ ((xrow >> 1) & 7)) * 16));
        int k0 = ks * 16 + trh * 8 + q4;
        s16x4 a0 = lds_tr(wl + k0 * 128 + ((wchunk ^ (((k0 >> 1) & 1) << 2)) * 16) + (p4 & 1) * 8);
        s16x4 a1 = lds_tr(wl + (k0 + 4) * 128 + ((wchunk ^ (((k0 >> 1) & 1) << 2)) * 16) + (p4 & 1) * 8);
        acc = MFMA32(cat_tr(a0, a1), b, acc);
      }
      const int key = m0 + wm * 32 + r;
      const int t = key >> 5;
      if (wn == 0) {
        bf16_t* kf = (bf16_t*)(ws + WS_KF);
        {
          float n2 = 0.f;
#pragma unroll
          for (int i = 0; i < 16; ++i) { const float kv = rbf(acc[i]); n2 = fmaf(kv, kv, n2); }
          n2 = wave_max_nonneg(half_sum(n2));
          if (lane == 0) st4_wt((float*)(ws + WS_KN) + t, __float_as_uint(n2));
        }
#pragma unroll
        for (int g = 0; g < 4; ++g) {
          const float e0 = (g == 3 && h == 1) ? 1.0f : acc[4 * g];
          u32x2 v = {pk2(e0, acc[4 * g + 1]), pk2(acc[4 * g + 2], acc[4 * g + 3])};
          st8_wt(kf + ((size_t)((t * 2 + (g >> 1)) * 64 + (g & 1) * 32 + r) * 8 + 4 * h), v);
        }
      } else {
        bf16_t* vt = (bf16_t*)(smem + 64 * 896 + 448 * 128) + wm * 1024;
        const int st = r >> 4, jj = ((r >> 3) & 1) * 4 + (r & 3), hh = (r >> 2) & 1;
#pragma unroll
        for (int g = 0; g < 4; ++g) {
#pragma unroll
          for (int e = 0; e < 4; ++e) {
            const int d = 8 * g + 4 * h + e;
            float val = acc[4 * g + e];
            if (d == 28) val = 1.0f;
            vt[(st * 64 + hh * 32 + d) * 8 + jj] = (__bf16)val;
          }
        }
        WAIT_LGKM0();
        bf16_t* vf = (bf16_t*)(ws + WS_VF) + (size_t)t * 1024;
#pragma unroll
        for (int i = 0; i < 2; ++i) st16_wt(vf + (i * 64 + lane) * 8, *(const u32x4*)(vt + (i * 64 + lane) * 8));
      }
    } else {
      const int t2 = tid - 256, row = t2 >> 2, qq = t2 & 3;
      const bf16_t* xr = x + (size_t)(m0 + row) * D_ + qq * 112;
      const bf16_t* gw = gate_w + qq * 112;
      float sum = 0.f;
#pragma unroll 2
      for (int c = 0; c < 14; ++c) {
        u32x4 xv = *(const u32x4*)(xr + c * 8);
        u32x4 gv = *(const u32x4*)(gw + c * 8);
#pragma unroll
        for (int e = 0; e < 4; ++e) {
          sum += rbf(bflo(xv[e]) * bflo(gv[e]));
          sum += rbf(bfhi(xv[e]) * bfhi(gv[e]));
        }
      }
      sum += __shfl_xor(sum, 1);
      sum += __shfl_xor(sum, 2);
      if (qq == 0) {
        float z = rbf(sum);
        float gsig = rbf(1.0f / (1.0f + __expf(-z)));
        st4_wt((float*)(ws + WS_G) + m0 + row, __float_as_uint(gsig));
      }
    }
  }
}

constexpr int K2_VOFF = 4 * 8192;
constexpr int K2_ROFF = 8 * 8192;
constexpr int K2_KVOFF = K2_ROFF + 8 * 1792;
constexpr int K2_POFF = K2_ROFF + 53 * 1024;
constexpr int K2_BOUT = K2_POFF + 2048;
constexpr int K2_LDS = K2_BOUT + 8 * 896;

__global__ __launch_bounds__(768) void k2_attn(char* __restrict__ ws) {
  extern __shared__ __attribute__((aligned(16))) char smem[];
  const int tid = threadIdx.x, lane = tid & 63, w = __builtin_amdgcn_readfirstlane(tid >> 6);
  const int r = lane & 31, h = lane >> 5;
  const int pos0 = blockIdx.x * 8;
  const int qg = w & 3, kh = (w >> 2) & 1;
  if (w >= 8) {
    const int li = w - 8;
    const bf16_t* P = (const bf16_t*)(ws + WS_P);
    const unsigned lbase = lds_off(smem);
    const bf16_t* kf = (const bf16_t*)(ws + WS_KF) + li * 512 + lane * 8;
    const bf16_t* vf = (const bf16_t*)(ws + WS_VF) + li * 512 + lane * 8;
    auto issueK = [&](int s) {
      glds16(kf + (size_t)(s * 4) * 512, lbase + (s & 3) * 8192 + li * 1024);
      glds16(kf + (size_t)(64 + s * 4) * 512, lbase + (s & 3) * 8192 + 4096 + li * 1024);
    };
    auto issueV = [&](int s) {
      glds16(vf + (size_t)(s * 4) * 512, lbase + K2_VOFF + (s & 3) * 8192 + li * 1024);
      glds16(vf + (size_t)(64 + s * 4) * 512, lbase + K2_VOFF + (s & 3) * 8192 + 4096 + li * 1024);
    };
    issueK(0);
#pragma unroll
    for (int i = 0; i < 4; ++i) {
      int p = li * 4 + i;
      p = p < 14 ? p : 13;
      const int o = p * 1024 + lane * 16, row = o / 1792, col = o - row * 1792;
      glds16(P + (size_t)(pos0 + row) * NP + (col >> 1), lbase + K2_ROFF + p * 1024);
    }
    glds4((const float*)(ws + WS_KN) + lane, lbase + K2_POFF);
    issueK(1); issueV(0);
    WAIT_VM(4);
    barrier_raw();
    issueK(2); issueV(1); issueK(3); issueV(2);
#pragma unroll 1
    for (int s = 0; s < 16; ++s) {
      if (s < 13) WAIT_VM(8); else if (s == 13) WAIT_VM(4); else WAIT_VM(0);
      barrier_raw();
      if (s < 3) {
#pragma unroll 1
        for (int i = (s ? 1 + 3 * s : 0); i < 4 + 3 * s; ++i) {
          int p = 14 + li * 10 + i;
          p = p > 52 ? 52 : p;
          int o = p * 1024 + lane * 16;
          o = o < 53760 ? o : 53760 - 16;
          const int row = o / 1792, col = o - row * 1792;
          int prow = pos0 - 22 + row;
          prow = prow < 0 ? 0 : prow;
          glds16(P + (size_t)prow * NP + 896 + (col >> 1), lbase + K2_ROFF + p * 1024);
        }
      }
      if (s < 12) issueK(s + 4);
      if (s < 13) issueV(s + 3);
    }
    barrier_raw(); barrier_raw(); barrier_raw();
    return;
  }
  barrier_raw();
  bf16x8 q0, q1;
  {
    const int qi = qg * 32 + r;
    const char* qp = smem + K2_ROFF + (qi & 7) * 1792 + (qi >> 3) * 56 + 16 * h;
    u32x2 a = *(const u32x2*)qp, b = *(const u32x2*)(qp + 8), c = *(const u32x2*)(qp + 32), d = *(const u32x2*)(qp + (h ? 32 : 40));
    if (h) d = (u32x2){0u, 0u};
    u32x4 t0 = {a[0], a[1], b[0], b[1]}, t1 = {c[0], c[1], d[0], d[1]};
    q0 = __builtin_bit_cast(bf16x8, t0); q1 = __builtin_bit_cast(bf16x8, t1);
  }
  f32x16 sa0, sa1, sb0, sb1;
  {
    const char* kb = smem + kh * 4096 + lane * 16;
    bf16x8 k00 = lds_ld16(kb), k01 = lds_ld16(kb + 1024), k10 = lds_ld16(kb + 2048), k11 = lds_ld16(kb + 3072);
    sa0 = MFMA32(k00, q0, (f32x16){}); sa1 = MFMA32(k10, q0, (f32x16){});
    sa0 = MFMA32(k01, q1, sa0); sa1 = MFMA32(k11, q1, sa1);
  }
  float sbound;
  {
    float n2 = 0.f;
#pragma unroll
    for (int i = 0; i < 8; ++i) { n2 = fmaf((float)q0[i], (float)q0[i], n2); n2 = fmaf((float)q1[i], (float)q1[i], n2); }
    n2 = half_sum(n2);
    const float kn2 = wave_max_nonneg(((const float*)(smem + K2_POFF))[lane]);
    sbound = sqrtf(n2 * kn2) * 1.01f + 1.0f;
  }
  bool fast = false;
  f32x16 o = {};
  float mref = 0.f;
  auto iter = [&](f32x16& c0, f32x16& c1, f32x16& n0, f32x16& n1, const int s) __attribute__((always_inline)) {
    barrier_raw();
    const char* kb = smem + ((s + 1) & 3) * 8192 + kh * 4096 + lane * 16;
    const char* vb = smem + K2_VOFF + (s & 3) * 8192 + kh * 4096 + lane * 16;
    bf16x8 k00 = lds_ld16(kb), k01 = lds_ld16(kb + 1024), k10 = lds_ld16(kb + 2048), k11 = lds_ld16(kb + 3072);
    bf16x8 v00 = lds_ld16(vb), v01 = lds_ld16(vb + 1024), v10 = lds_ld16(vb + 2048), v11 = lds_ld16(vb + 3072);
    __builtin_amdgcn_sched_barrier(0);
    if (!fast) {
      float pmax = -3.0e38f;
#pragma unroll
      for (int i = 0; i < 16; i += 2) pmax = fmaxf(fmaxf(pmax, c0[i]), c0[i + 1]);
#pragma unroll
      for (int i = 0; i < 16; i += 2) pmax = fmaxf(fmaxf(pmax, c1[i]), c1[i + 1]);
      {
        auto rr = __builtin_amdgcn_permlane32_swap(__float_as_uint(pmax), __float_as_uint(pmax), false, false);
        pmax = fmaxf(fmaxf(pmax, __uint_as_float(rr[0])), __uint_as_float(rr[1]));
      }
      if (s == 0 || !__all(pmax <= THR_L2)) {
        const float mnew = rbf(mref + (s == 0 ? pmax : fmaxf(pmax, 0.f)));
        const float delta = mnew - mref;
        mref = mnew;
        if (s != 0) {
          const float alpha = __builtin_amdgcn_exp2f(-delta);
#pragma unroll
          for (int i = 0; i < 16; ++i) o[i] *= alpha;
        }
#pragma unroll
        for (int i = 0; i < 16; ++i) { c0[i] -= delta; c1[i] -= delta; }
        if (h) q1[4] = (__bf16)(-mnew);
      }
      if (s == 0) fast = __all(sbound - mref <= 64.0f);
    }
#pragma unroll
    for (int i = 0; i < 8; ++i) c0[i] = __builtin_amdgcn_exp2f(c0[i]);
    __builtin_amdgcn_sched_barrier(0);
    n0 = MFMA32(k00, q0, (f32x16){}); n1 = MFMA32(k10, q0, (f32x16){});
#pragma unroll
    for (int i = 8; i < 16; ++i) c0[i] = __builtin_amdgcn_exp2f(c0[i]);
    u32x4 p00 = {pk2(c0[0], c0[1]), pk2(c0[2], c0[3]), pk2(c0[4], c0[5]), pk2(c0[6], c0[7])};
    u32x4 p01 = {pk2(c0[8], c0[9]), pk2(c0[10], c0[11]), pk2(c0[12], c0[13]), pk2(c0[14], c0[15])};
    __builtin_amdgcn_sched_barrier(0);
    n0 = MFMA32(k01, q1, n0); n1 = MFMA32(k11, q1, n1);
#pragma unroll
    for (int i = 0; i < 8; ++i) c1[i] = __builtin_amdgcn_exp2f(c1[i]);
    __builtin_amdgcn_sched_barrier(0);
    o = MFMA32(v00, __builtin_bit_cast(bf16x8, p00), o);
    o = MFMA32(v01, __builtin_bit_cast(bf16x8, p01), o);
#pragma unroll
    for (int i = 8; i < 16; ++i) c1[i] = __builtin_amdgcn_exp2f(c1[i]);
    u32x4 p10 = {pk2(c1[0], c1[1]), pk2(c1[2], c1[3]), pk2(c1[4], c1[5]), pk2(c1[6], c1[7])};
    u32x4 p11 = {pk2(c1[8], c1[9]), pk2(c1[10], c1[11]), pk2(c1[12], c1[13]), pk2(c1[14], c1[15])};
    __builtin_amdgcn_sched_barrier(0);
    o = MFMA32(v10, __builtin_bit_cast(bf16x8, p10), o);
    o = MFMA32(v11, __builtin_bit_cast(bf16x8, p11), o);
  };
#pragma unroll
  for (int s = 0; s < 16; s += 2) {
    iter(sa0, sa1, sb0, sb1, s);
    iter(sb0, sb1, sa0, sa1, s + 1);
  }
  barrier_raw();
  char* outt = smem + 40960;
  {
    char* red = smem + (qg * 2 + kh) * 5120 + lane * 16;
#pragma unroll
    for (int g = 0; g < 4; ++g) *(f32x4*)(red + g * 1024) = (f32x4){o[4 * g], o[4 * g + 1], o[4 * g + 2], o[4 * g + 3]};
    *(float*)(smem + (qg * 2 + kh) * 5120 + 4096 + lane * 4) = mref;
  }
  {
    const int pl = w, hd = lane >> 2, sub = lane & 3;
    const char* qb = smem + K2_ROFF + pl * 1792 + 896 + hd * 56;
    const char* kb = smem + K2_KVOFF + (14 + pl - 2 * sub) * 1792 + hd * 56;
    float acc0 = 0.f, acc1 = 0.f;
#pragma unroll
    for (int i = 0; i < 7; ++i) {
      const u32x2 qv = *(const u32x2*)(qb + i * 8), ka = *(const u32x2*)(kb + i * 8), kc = *(const u32x2*)(kb - 8 * 1792 + i * 8);
      const float q0f = bflo(qv[0]), q1f = bfhi(qv[0]), q2f = bflo(qv[1]), q3f = bfhi(qv[1]);
      acc0 = fmaf(q0f, bflo(ka[0]), acc0); acc0 = fmaf(q1f, bfhi(ka[0]), acc0);
      acc0 = fmaf(q2f, bflo(ka[1]), acc0); acc0 = fmaf(q3f, bfhi(ka[1]), acc0);
      acc1 = fmaf(q0f, bflo(kc[0]), acc1); acc1 = fmaf(q1f, bfhi(kc[0]), acc1);
      acc1 = fmaf(q2f, bflo(kc[1]), acc1); acc1 = fmaf(q3f, bfhi(kc[1]), acc1);
    }
    float lv0 = rbf(rbf(acc0) * SCALE_), lv1 = rbf(rbf(acc1) * SCALE_);
    if (pos0 + pl - 2 * sub < 0) lv0 = rbf(-1e9f);
    if (pos0 + pl - 2 * sub - 8 < 0) lv1 = rbf(-1e9f);
    float mx = fmaxf(lv0, lv1);
    mx = fmaxf(mx, dpp_f<0xB1>(mx));
    mx = fmaxf(mx, dpp_f<0x4E>(mx));
    const float e0 = __expf(lv0 - mx), e1 = __expf(lv1 - mx);
    float sum = e0 + e1;
    sum += dpp_f<0xB1>(sum);
    sum += dpp_f<0x4E>(sum);
    const float inv = 1.0f / sum;
    const float p0 = rbf(e0 * inv), p1 = rbf(e1 * inv);
    float pw[8];
    pw[0] = dpp_f<0x00>(p0); pw[1] = dpp_f<0x55>(p0); pw[2] = dpp_f<0xAA>(p0); pw[3] = dpp_f<0xFF>(p0);
    pw[4] = dpp_f<0x00>(p1); pw[5] = dpp_f<0x55>(p1); pw[6] = dpp_f<0xAA>(p1); pw[7] = dpp_f<0xFF>(p1);
    const int ca = hd * 56 + sub * 8, cb = hd * 56 + (sub < 3 ? sub + 4 : sub) * 8;
    float oa[4] = {0.f, 0.f, 0.f, 0.f}, ob[4] = {0.f, 0.f, 0.f, 0.f};
#pragma unroll
    for (int ww = 0; ww < 8; ++ww) {
      const char* vr = smem + K2_KVOFF + (14 + pl - 2 * ww) * 1792 + 896;
      const u32x2 va = *(const u32x2*)(vr + ca), vb = *(const u32x2*)(vr + cb);
      oa[0] = fmaf(pw[ww], bflo(va[0]), oa[0]); oa[1] = fmaf(pw[ww], bfhi(va[0]), oa[1]);
      oa[2] = fmaf(pw[ww], bflo(va[1]), oa[2]); oa[3] = fmaf(pw[ww], bfhi(va[1]), oa[3]);
      ob[0] = fmaf(pw[ww], bflo(vb[0]), ob[0]); ob[1] = fmaf(pw[ww], bfhi(vb[0]), ob[1]);
      ob[2] = fmaf(pw[ww], bflo(vb[1]), ob[2]); ob[3] = fmaf(pw[ww], bfhi(vb[1]), ob[3]);
    }
    *(u32x2*)(smem + K2_BOUT + pl * 896 + ca) = (u32x2){pk2(oa[0], oa[1]), pk2(oa[2], oa[3])};
    if (sub < 3) *(u32x2*)(smem + K2_BOUT + pl * 896 + cb) = (u32x2){pk2(ob[0], ob[1]), pk2(ob[2], ob[3])};
  }
  WAIT_LGKM0();
  barrier_raw();
  {
    const char* base = smem + qg * 2 * 5120;
    const float m0_ = *(const float*)(base + 4096 + lane * 4), m1_ = *(const float*)(base + 5120 + 4096 + lane * 4);
    const float l0 = *(const float*)(base + 3 * 1024 + (32 + r) * 16), l1 = *(const float*)(base + 5120 + 3 * 1024 + (32 + r) * 16);
    const float mn = fmaxf(m0_, m1_);
    float a0 = __builtin_amdgcn_exp2f(m0_ - mn), a1 = __builtin_amdgcn_exp2f(m1_ - mn);
    const float inv = 1.0f / (l0 * a0 + l1 * a1);
    a0 *= inv; a1 *= inv;
    const int qi = qg * 32 + r;
#pragma unroll
    for (int gg = 0; gg < 2; ++gg) {
      const int g = 2 * kh + gg;
      const f32x4 acc = *(const f32x4*)(base + g * 1024 + lane * 16) * a0 + *(const f32x4*)(base + 5120 + g * 1024 + lane * 16) * a1;
      if (!(g == 3 && h == 1))
        *(u32x2*)(outt + (qi & 7) * 896 + (qi >> 3) * 56 + 8 * h + 16 * g) = (u32x2){pk2(acc[0], acc[1]), pk2(acc[2], acc[3])};
    }
  }
  WAIT_LGKM0();
  barrier_raw();
  if (lane < 56) {
    char* dst = ws + WS_OAB + (size_t)(pos0 + w) * 1792 + lane * 16;
    st16_wt(dst, *(const u32x4*)(outt + w * 896 + lane * 16));
    st16_wt(dst + 896, *(const u32x4*)(smem + K2_BOUT + w * 896 + lane * 16));
  }
}

constexpr int K3_STAGE = 32768;
constexpr int K3_LDS = 4 * K3_STAGE;

__global__ __launch_bounds__(512) void k3_out(const bf16_t* __restrict__ woa, const bf16_t* __restrict__ wob,
                                              const char* __restrict__ ws, bf16_t* __restrict__ out) {
  extern __shared__ __attribute__((aligned(16))) char smem[];
  const int tid = threadIdx.x, lane = tid & 63, w = __builtin_amdgcn_readfirstlane(tid >> 6);
  const int r = lane & 31, h = lane >> 5;
  const int bid = blockIdx.x;
  const int xcd = bid & 7, idx = bid >> 3;
  const int mt = xcd * 4 + idx / 7, nt = idx % 7;
  const int m0 = mt * 64, n0 = nt * 64;
  const int br = w >> 2, wl = w & 3;
  const bf16_t* oab = (const bf16_t*)(ws + WS_OAB);
  const bf16_t* wo = br ? wob : woa;
  const bf16_t* osrc[2];
  const bf16_t* wsrc[2];
#pragma unroll
  for (int i = 0; i < 2; ++i) {
    int row = (wl * 2 + i) * 8 + (lane >> 3), slot = lane & 7;
    osrc[i] = oab + (size_t)(m0 + row) * 896 + br * 448 + ((slot ^ ((row >> 1) & 7)) * 8);
    wsrc[i] = wo + (size_t)row * 448 + n0 + ((slot ^ (((row >> 1) & 1) << 2)) * 8);
  }
  auto issue = [&](int s) {
    const unsigned base = lds_off(smem) + (s & 3) * K3_STAGE + br * 16384;
#pragma unroll
    for (int i = 0; i < 2; ++i) {
      glds16(osrc[i] + s * 64, base + (wl * 2 + i) * 1024);
      glds16(wsrc[i] + (size_t)s * 64 * 448, base + 8192 + (wl * 2 + i) * 1024);
    }
  };
  issue(0); issue(1); issue(2);
  const int wn = wl & 1, wm = wl >> 1;
  const int orow = wm * 32 + r;
  const int g16 = lane >> 4, q4 = (lane & 15) >> 2, p4 = lane & 3;
  const int trh = g16 >> 1, trb = g16 & 1;
  const int wchunk = wn * 4 + trb * 2 + (p4 >> 1);
  f32x16 acc = {};
#pragma unroll
  for (int s = 0; s < 7; ++s) {
    if (s < 5) WAIT_VM(8); else if (s == 5) WAIT_VM(4); else WAIT_VM(0);
    barrier_raw();
    if (s + 3 < 7) issue(s + 3);
    const char* ob = smem + (s & 3) * K3_STAGE + br * 16384;
    const char* wb = ob + 8192;
#pragma unroll
    for (int ks = 0; ks < 4; ++ks) {
      bf16x8 b = lds_ld16(ob + orow * 128 + (((2 * ks + h) ^ ((orow >> 1) & 7)) * 16));
      int k0 = ks * 16 + trh * 8 + q4;
      s16x4 a0 = lds_tr(wb + k0 * 128 + ((wchunk ^ (((k0 >> 1) & 1) << 2)) * 16) + (p4 & 1) * 8);
      s16x4 a1 = lds_tr(wb + (k0 + 4) * 128 + ((wchunk ^ (((k0 >> 1) & 1) << 2)) * 16) + (p4 & 1) * 8);
      acc = MFMA32(cat_tr(a0, a1), b, acc);
    }
  }
  barrier_raw();
  float* red = (float*)smem + wl * (16 * 64);
  if (br == 1) {
#pragma unroll
    for (int i = 0; i < 16; ++i) red[i * 64 + lane] = acc[i];
  }
  WAIT_LGKM0();
  barrier_raw();
  char* ot = smem + 32768;
  if (br == 0) {
    const int m = m0 + orow;
    const float g = ((const float*)(ws + WS_G))[m];
    const float omg = rbf(1.0f - g);
#pragma unroll
    for (int gq = 0; gq < 4; ++gq) {
      float res[4];
#pragma unroll
      for (int e = 0; e < 4; ++e) {
        float a = rbf(acc[4 * gq + e]);
        float b = rbf(red[(4 * gq + e) * 64 + lane]);
        res[e] = rbf(g * a) + rbf(omg * b);
      }
      u32x2 v = {pk2(res[0], res[1]), pk2(res[2], res[3])};
      *(u32x2*)(ot + orow * 144 + (wn * 32 + 8 * gq + 4 * h) * 2) = v;
    }
  }
  WAIT_LGKM0();
  barrier_raw();
  {
    const int row = w * 8 + (lane >> 3), c = lane & 7;
    u32x4 v = *(const u32x4*)(ot + row * 144 + c * 16);
    st16_wt(out + (size_t)(m0 + row) * D_ + n0 + c * 8, v);
  }
}

extern "C" void kernel_launch(void* const* d_in, const int* in_sizes, int n_in, void* d_out, int out_size, void* d_ws,
                              size_t ws_size, hipStream_t stream) {
  const bf16_t* x = (const bf16_t*)d_in[0];
  const bf16_t* wq = (const bf16_t*)d_in[1];
  const bf16_t* wk = (const bf16_t*)d_in[2];
  const bf16_t* wv = (const bf16_t*)d_in[3];
  const bf16_t* woa = (const bf16_t*)d_in[4];
  const bf16_t* wqkv = (const bf16_t*)d_in[6];
  const bf16_t* wob = (const bf16_t*)d_in[7];
  const bf16_t* gw = (const bf16_t*)d_in[8];
  char* ws = (char*)d_ws;
  (void)hipFuncSetAttribute((const void*)k1_proj, hipFuncAttributeMaxDynamicSharedMemorySize, K1_LDS);
  (void)hipFuncSetAttribute((const void*)k2_attn, hipFuncAttributeMaxDynamicSharedMemorySize, K2_LDS);
  (void)hipFuncSetAttribute((const void*)k3_out, hipFuncAttributeMaxDynamicSharedMemorySize, K3_LDS);
  hipLaunchKernelGGL(k1_proj, dim3(256), dim3(512), K1_LDS, stream, x, wq, wk, wv, wqkv, gw, ws);
  hipLaunchKernelGGL(k2_attn, dim3(256), dim3(768), K2_LDS, stream, ws);
  hipLaunchKernelGGL(k3_out, dim3(224), dim3(512), K3_LDS, stream, woa, wob, (const char*)ws, (bf16_t*)d_out);
}
```

```cpp
#include <hip/hip_runtime.h>
#include <hip/hip_bf16.h>
#include <stdint.h>

typedef __bf16 bf16_t;
typedef __bf16 bf16x8 __attribute__((ext_vector_type(8)));
typedef __bf16 bf16x4 __attribute__((ext_vector_type(4)));
typedef __bf16 bf16x2 __attribute__((ext_vector_type(2)));
typedef short s16x4 __attribute__((ext_vector_type(4)));
typedef float f32x16 __attribute__((ext_vector_type(16)));
typedef float f32x2 __attribute__((ext_vector_type(2)));
typedef float f32x4 __attribute__((ext_vector_type(4)));
typedef unsigned u32x2 __attribute__((ext_vector_type(2)));
typedef unsigned u32x4 __attribute__((ext_vector_type(4)));

#define LDS_AS __attribute__((address_space(3)))
#define GLB_AS __attribute__((address_space(1)))

constexpr int S_ = 2048, D_ = 448, H_ = 16, DH_ = 28;
constexpr int NQKV = 3 * D_;

constexpr int NP = 1792;
constexpr size_t WS_P = 0;
constexpr size_t WS_KF = WS_P + (size_t)S_ * NP * 2;
constexpr size_t WS_VF = WS_KF + (size_t)S_ * 32 * 2;
constexpr size_t WS_OAB = WS_VF + (size_t)S_ * 32 * 2;
constexpr size_t WS_G = WS_OAB + (size_t)S_ * 896 * 2;
constexpr size_t WS_KN = WS_G + (size_t)S_ * 4;
constexpr size_t WS_END = WS_KN + 256;

constexpr float SCALE_ = 0.1889822365046136f;
constexpr float CL2 = SCALE_ * 1.4426950408889634f;
constexpr float THR_L2 = 8.0f * 1.4426950408889634f;

__device__ __forceinline__ void glds16(const void* gsrc, unsigned lds_dst) {
  unsigned keep;
  asm volatile("s_mov_b32 %0, m0\n\ts_mov_b32 m0, %2\n\ts_nop 0\n\tglobal_load_lds_dwordx4 %1, off\n\ts_mov_b32 m0, %0"
               : "=&s"(keep) : "v"(gsrc), "s"(lds_dst) : "memory");
}
__device__ __forceinline__ void glds4(const void* gsrc, unsigned lds_dst) {
  unsigned keep;
  asm volatile("s_mov_b32 %0, m0\n\ts_mov_b32 m0, %2\n\ts_nop 0\n\tglobal_load_lds_dword %1, off\n\ts_mov_b32 m0, %0"
               : "=&s"(keep) : "v"(gsrc), "s"(lds_dst) : "memory");
}
__device__ __forceinline__ unsigned lds_off(const void* p) { return (unsigned)(size_t)(const LDS_AS char*)p; }
__device__ __forceinline__ void st16_wt(void* p, u32x4 v) {
  asm volatile("global_store_dwordx4 %0, %1, off sc1\n\ts_nop 1" ::"v"(p), "v"(v) : "memory");
}
__device__ __forceinline__ void st8_wt(void* p, u32x2 v) {
  asm volatile("global_store_dwordx2 %0, %1, off sc1\n\ts_nop 1" ::"v"(p), "v"(v) : "memory");
}
__device__ __forceinline__ void st4_wt(void* p, unsigned v) {
  asm volatile("global_store_dword %0, %1, off sc1\n\ts_nop 1" ::"v"(p), "v"(v) : "memory");
}
template <int CTRL> __device__ __forceinline__ float dpp_f(float x) {
  return __uint_as_float((unsigned)__builtin_amdgcn_update_dpp(0, (int)__float_as_uint(x), CTRL, 0xf, 0xf, true));
}
__device__ __forceinline__ float wave_max_nonneg(float v) {
  v = fmaxf(v, dpp_f<0xB1>(v));
  v = fmaxf(v, dpp_f<0x4E>(v));
  v = fmaxf(v, dpp_f<0x141>(v));
  v = fmaxf(v, dpp_f<0x140>(v));
  const float a = __uint_as_float(__builtin_amdgcn_readlane(__float_as_uint(v), 0));
  const float b = __uint_as_float(__builtin_amdgcn_readlane(__float_as_uint(v), 16));
  const float c = __uint_as_float(__builtin_amdgcn_readlane(__float_as_uint(v), 32));
  const float d = __uint_as_float(__builtin_amdgcn_readlane(__float_as_uint(v), 48));
  return fmaxf(fmaxf(a, b), fmaxf(c, d));
}
__device__ __forceinline__ float half_sum(float v) {
  auto rr = __builtin_amdgcn_permlane32_swap(__float_as_uint(v), __float_as_uint(v), false, false);
  return __uint_as_float(rr[0]) + __uint_as_float(rr[1]);
}
#define WAIT_VM(n) asm volatile("s_waitcnt vmcnt(" #n ")" ::: "memory")
#define WAIT_LGKM0() asm volatile("s_waitcnt lgkmcnt(0)" ::: "memory")
__device__ __forceinline__ void barrier_raw() {
  asm volatile("" ::: "memory");
  __builtin_amdgcn_s_barrier();
  asm volatile("" ::: "memory");
}
__device__ __forceinline__ bf16x8 lds_ld16(const char* p) { return *(const bf16x8*)p; }
__device__ __forceinline__ s16x4 lds_tr(const char* p) {
  return __builtin_amdgcn_ds_read_tr16_b64_v4i16((LDS_AS s16x4*)p);
}
__device__ __forceinline__ bf16x8 cat_tr(s16x4 a, s16x4 b) {
  typedef short s16x8 __attribute__((ext_vector_type(8)));
  s16x8 r = {a[0], a[1], a[2], a[3], b[0], b[1], b[2], b[3]};
  return __builtin_bit_cast(bf16x8, r);
}
__device__ __forceinline__ unsigned pk2(float a, float b) {
  f32x2 v = {a, b};
  bf16x2 r = __builtin_convertvector(v, bf16x2);
  return __builtin_bit_cast(unsigned, r);
}
__device__ __forceinline__ float rbf(float a) { return (float)(__bf16)a; }
__device__ __forceinline__ float bf2f(unsigned short u) { return __uint_as_float(((unsigned)u) << 16); }
__device__ __forceinline__ float bflo(unsigned u) { return __uint_as_float(u << 16); }
__device__ __forceinline__ float bfhi(unsigned u) { return __uint_as_float(u & 0xffff0000u); }
#define MFMA32(a, b, c) __builtin_amdgcn_mfma_f32_32x32x16_bf16((a), (b), (c), 0, 0, 0)

constexpr int K1_WOFF = 7 * 16384;
constexpr int K1_SOFF = K1_WOFF + 3 * 8192;
constexpr int K1_LDS = K1_SOFF + 128 * 144;

__global__ __launch_bounds__(512) void k1_proj(const bf16_t* __restrict__ x, const bf16_t* __restrict__ wq,
                                               const bf16_t* __restrict__ wk, const bf16_t* __restrict__ wv,
                                               const bf16_t* __restrict__ wqkv, const bf16_t* __restrict__ gate_w,
                                               char* __restrict__ ws) {
  extern __shared__ __attribute__((aligned(16))) char smem[];
  const int tid = threadIdx.x, lane = tid & 63, w = __builtin_amdgcn_readfirstlane(tid >> 6);
  const int r = lane & 31, h = lane >> 5;
  const int bid = blockIdx.x;
  if (bid < 224) {
    const int xcd = bid & 7, idx = bid >> 3;
    const int mt = (xcd >> 1) * 4 + idx / 7, nt = (xcd & 1) * 7 + idx % 7;
    const int m0 = mt * 128, n0 = nt * 128;
    const unsigned lb = lds_off(smem);
    const bf16_t* xsrc[2];
#pragma unroll
    for (int i = 0; i < 2; ++i) {
      const int row = (w * 2 + i) * 8 + (lane >> 3), slot = lane & 7;
      xsrc[i] = x + (size_t)(m0 + row) * D_ + ((slot ^ ((row >> 1) & 7)) * 8);
    }
    const bf16_t* wsrc[2];
    int wstr[2];
#pragma unroll
    for (int hf = 0; hf < 2; ++hf) {
      const int krow = w * 8 + (lane >> 3), slot = lane & 7;
      const int vc = n0 + hf * 64;
      const int cc = (slot ^ (((krow >> 1) & 1) << 2)) * 8;
      wstr[hf] = vc < 448 ? 448 : NQKV;
      wsrc[hf] = (vc < 448 ? wq + vc : wqkv + (vc - 448)) + (size_t)krow * wstr[hf] + cc;
    }
    auto issueX = [&](int c) {
      glds16(xsrc[0] + c * 64, lb + c * 16384 + (w * 2) * 1024);
      glds16(xsrc[1] + c * 64, lb + c * 16384 + (w * 2 + 1) * 1024);
    };
    auto issueW = [&](int t) {
      const int hf = t / 7, c = t % 7;
      glds16(wsrc[hf] + (size_t)c * 64 * wstr[hf], lb + K1_WOFF + (t % 3) * 8192 + w * 1024);
    };
    issueX(0); issueW(0); issueX(1); issueW(1); issueX(2);
    const int wn = w & 1, wm = w >> 1;
    const int xrow = wm * 32 + r;
    const int g16 = lane >> 4, q4 = (lane & 15) >> 2, p4 = lane & 3;
    const int trh = g16 >> 1, trb = g16 & 1;
    const int wchunk = wn * 4 + trb * 2 + (p4 >> 1);
    bf16_t* P = (bf16_t*)(ws + WS_P);
    char* stg = smem + K1_SOFF;
    auto stage_out = [&](const f32x16& acc, int hf) {
      const float sc = (n0 + hf * 64 + wn * 32 < 448) ? CL2 : 1.0f;
#pragma unroll
      for (int g = 0; g < 4; ++g) {
        u32x2 v = {pk2(acc[4 * g] * sc, acc[4 * g + 1] * sc), pk2(acc[4 * g + 2] * sc, acc[4 * g + 3] * sc)};
        *(u32x2*)(stg + xrow * 144 + (wn * 32 + 8 * g + 4 * h) * 2) = v;
      }
      WAIT_LGKM0();
    };
    auto store_out = [&](int hf) {
#pragma unroll
      for (int i = 0; i < 2; ++i) {
        const int ml = (w * 2 + i) * 8 + (lane >> 3), c = lane & 7;
        st16_wt(P + (size_t)(m0 + ml) * NP + n0 + hf * 64 + c * 8, *(const u32x4*)(stg + ml * 144 + c * 16));
      }
    };
    f32x16 acc = {};
#pragma unroll
    for (int t = 0; t < 14; ++t) {
      if (t < 5) WAIT_VM(5);
      else if (t == 5 || t == 8 || t == 9) WAIT_VM(3);
      else if (t < 13) WAIT_VM(1);
      else WAIT_VM(0);
      barrier_raw();
      if (t < 12) issueW(t + 2);
      if (t < 4) issueX(t + 3);
      if (t == 7) store_out(0);
      const char* xb = smem + (t % 7) * 16384;
      const char* wb = smem + K1_WOFF + (t % 3) * 8192;
#pragma unroll
      for (int ks = 0; ks < 4; ++ks) {
        bf16x8 b = lds_ld16(xb + xrow * 128 + (((2 * ks + h) ^ ((xrow >> 1) & 7)) * 16));
        const int k0 = ks * 16 + trh * 8 + q4;
        s16x4 a0 = lds_tr(wb + k0 * 128 + ((wchunk ^ (((k0 >> 1) & 1) << 2)) * 16) + (p4 & 1) * 8);
        s16x4 a1 = lds_tr(wb + (k0 + 4) * 128 + ((wchunk ^ (((k0 >> 1) & 1) << 2)) * 16) + (p4 & 1) * 8);
        acc = MFMA32(cat_tr(a0, a1), b, acc);
      }
      if (t == 6) { stage_out(acc, 0); acc = (f32x16){}; }
    }
    barrier_raw();
    stage_out(acc, 1);
    barrier_raw();
    store_out(1);
  } else {
    const int jb = bid - 224, xcd = jb & 7;
    const int j = (xcd >> 1) * 8 + (xcd & 1) * 4 + (jb >> 3), m0 = j * 64;
    char* xl = smem;
    char* wl = smem + 64 * 896;
#pragma unroll
    for (int i = 0; i < 7; ++i) {
      int sl = (w * 7 + i) * 64 + lane;
      int row = sl / 56, slot = sl - row * 56;
      glds16(x + (size_t)(m0 + row) * D_ + ((slot ^ ((row >> 1) & 7)) * 8), lds_off(xl) + (w * 7 + i) * 1024);
    }
    {
      u32x2 wv_[14];
      const int pc = tid & 15;
      const bf16_t* wsrc = (pc < 8 ? wk : wv) + ((pc & 7) < 7 ? (pc & 7) * 4 : 0);
#pragma unroll
      for (int i = 0; i < 14; ++i) {
        int row = (tid >> 4) + i * 32;
        wv_[i] = *(const u32x2*)(wsrc + row * 28);
      }
#pragma unroll
      for (int i = 0; i < 14; ++i) {
        int row = (tid >> 4) + i * 32;
        u32x2 v = ((pc & 7) < 7) ? wv_[i] : (u32x2){0u, 0u};
        int chunk = pc >> 1;
        *(u32x2*)(wl + row * 128 + ((chunk ^ (((row >> 1) & 1) << 2)) * 16) + (pc & 1) * 8) = v;
      }
    }
    WAIT_VM(0);
    WAIT_LGKM0();
    barrier_raw();
    if (w < 4) {
      const int wn = w & 1, wm = w >> 1;
      const int xrow = wm * 32 + r;
      const int g16 = lane >> 4, q4 = (lane & 15) >> 2, p4 = lane & 3;
      const int trh = g16 >> 1, trb = g16 & 1;
      const int wchunk = wn * 4 + trb * 2 + (p4 >> 1);
      f32x16 acc = {};
#pragma unroll 4
      for (int ks = 0; ks < 28; ++ks) {
        bf16x8 b = lds_ld16(xl + xrow * 896 + (((2 * ks + h) ^ ((xrow >> 1) & 7)) * 16));
        int k0 = ks * 16 + trh * 8 + q4;
        s16x4 a0 = lds_tr(wl + k0 * 128 + ((wchunk ^ (((k0 >> 1) & 1) << 2)) * 16) + (p4 & 1) * 8);
        s16x4 a1 = lds_tr(wl + (k0 + 4) * 128 + ((wchunk ^ (((k0 >> 1) & 1) << 2)) * 16) + (p4 & 1) * 8);
        acc = MFMA32(cat_tr(a0, a1), b, acc);
      }
      const int key = m0 + wm * 32 + r;
      const int t = key >> 5;
      if (wn == 0) {
        bf16_t* kf = (bf16_t*)(ws + WS_KF);
        {
          float n2 = 0.f;
#pragma unroll
          for (int i = 0; i < 16; ++i) { const float kv = rbf(acc[i]); n2 = fmaf(kv, kv, n2); }
          n2 = wave_max_nonneg(half_sum(n2));
          if (lane == 0) st4_wt((float*)(ws + WS_KN) + t, __float_as_uint(n2));
        }
#pragma unroll
        for (int g = 0; g < 4; ++g) {
          const float e0 = (g == 3 && h == 1) ? 1.0f : acc[4 * g];
          u32x2 v = {pk2(e0, acc[4 * g + 1]), pk2(acc[4 * g + 2], acc[4 * g + 3])};
          st8_wt(kf + ((size_t)((t * 2 + (g >> 1)) * 64 + (g & 1) * 32 + r) * 8 + 4 * h), v);
        }
      } else {
        bf16_t* vt = (bf16_t*)(smem + 64 * 896 + 448 * 128) + wm * 1024;
        const int st = r >> 4, jj = ((r >> 3) & 1) * 4 + (r & 3), hh = (r >> 2) & 1;
#pragma unroll
        for (int g = 0; g < 4; ++g) {
#pragma unroll
          for (int e = 0; e < 4; ++e) {
            const int d = 8 * g + 4 * h + e;
            float val = acc[4 * g + e];
            if (d == 28) val = 1.0f;
            vt[(st * 64 + hh * 32 + d) * 8 + jj] = (__bf16)val;
          }
        }
        WAIT_LGKM0();
        bf16_t* vf = (bf16_t*)(ws + WS_VF) + (size_t)t * 1024;
#pragma unroll
        for (int i = 0; i < 2; ++i) st16_wt(vf + (i * 64 + lane) * 8, *(const u32x4*)(vt + (i * 64 + lane) * 8));
      }
    } else {
      const int t2 = tid - 256, row = t2 >> 2, qq = t2 & 3;
      const bf16_t* xr = x + (size_t)(m0 + row) * D_ + qq * 112;
      const bf16_t* gw = gate_w + qq * 112;
      float sum = 0.f;
#pragma unroll 2
      for (int c = 0; c < 14; ++c) {
        u32x4 xv = *(const u32x4*)(xr + c * 8);
        u32x4 gv = *(const u32x4*)(gw + c * 8);
#pragma unroll
        for (int e = 0; e < 4; ++e) {
          sum += rbf(bflo(xv[e]) * bflo(gv[e]));
          sum += rbf(bfhi(xv[e]) * bfhi(gv[e]));
        }
      }
      sum += __shfl_xor(sum, 1);
      sum += __shfl_xor(sum, 2);
      if (qq == 0) {
        float z = rbf(sum);
        float gsig = rbf(1.0f / (1.0f + __expf(-z)));
        st4_wt((float*)(ws + WS_G) + m0 + row, __float_as_uint(gsig));
      }
    }
  }
}

constexpr int K2_VOFF = 4 * 8192;
constexpr int K2_ROFF = 8 * 8192;
constexpr int RS = 1824;
constexpr int K2_KVOFF = K2_ROFF + 8 * RS;
constexpr int K2_POFF = K2_ROFF + 30 * RS;
constexpr int K2_BOUT = K2_POFF + 2048;
constexpr int K2_LDS = K2_BOUT + 8 * 896;

__global__ __launch_bounds__(768) void k2_attn(char* __restrict__ ws) {
  extern __shared__ __attribute__((aligned(16))) char smem[];
  const int tid = threadIdx.x, lane = tid & 63, w = __builtin_amdgcn_readfirstlane(tid >> 6);
  const int r = lane & 31, h = lane >> 5;
  const int pos0 = blockIdx.x * 8;
  const int qg = w & 3, kh = (w >> 2) & 1;
  if (w >= 8) {
    const int li = w - 8;
    __builtin_amdgcn_s_setprio(3);
    const bf16_t* P = (const bf16_t*)(ws + WS_P);
    const unsigned lbase = lds_off(smem);
    const bf16_t* kf = (const bf16_t*)(ws + WS_KF) + li * 512 + lane * 8;
    const bf16_t* vf = (const bf16_t*)(ws + WS_VF) + li * 512 + lane * 8;
    auto issueK = [&](int s) {
      glds16(kf + (size_t)(s * 4) * 512, lbase + (s & 3) * 8192 + li * 1024);
      glds16(kf + (size_t)(64 + s * 4) * 512, lbase + (s & 3) * 8192 + 4096 + li * 1024);
    };
    auto issueV = [&](int s) {
      glds16(vf + (size_t)(s * 4) * 512, lbase + K2_VOFF + (s & 3) * 8192 + li * 1024);
      glds16(vf + (size_t)(64 + s * 4) * 512, lbase + K2_VOFF + (s & 3) * 8192 + 4096 + li * 1024);
    };
    issueK(0);
#pragma unroll
    for (int i = 0; i < 2; ++i) {
      const int row = li * 2 + i;
      const bf16_t* src = P + (size_t)(pos0 + row) * NP + lane * 8;
      glds16(src, lbase + K2_ROFF + row * RS);
      if (lane < 48) glds16(src + 512, lbase + K2_ROFF + row * RS + 1024);
    }
    glds4((const float*)(ws + WS_KN) + lane, lbase + K2_POFF);
    issueK(1); issueV(0);
    WAIT_VM(4);
    barrier_raw();
    issueK(2); issueV(1); issueK(3); issueV(2);
#pragma unroll 1
    for (int s = 0; s < 16; ++s) {
      if (s < 13) WAIT_VM(8); else if (s == 13) WAIT_VM(4); else WAIT_VM(0);
      barrier_raw();
      if (s < 6) {
        int row = li * 6 + s;
        row = row > 21 ? 21 : row;
        int prow = pos0 - 14 + row;
        prow = prow < 0 ? 0 : prow;
        const bf16_t* src = P + (size_t)prow * NP + 896 + lane * 8;
        glds16(src, lbase + K2_KVOFF + row * RS);
        if (lane < 48) glds16(src + 512, lbase + K2_KVOFF + row * RS + 1024);
      }
      if (s < 12) issueK(s + 4);
      if (s < 13) issueV(s + 3);
    }
    barrier_raw(); barrier_raw(); barrier_raw();
    return;
  }
  barrier_raw();
  bf16x8 q0, q1;
  {
    const int qi = qg * 32 + r;
    const char* qp = smem + K2_ROFF + (qi & 7) * RS + (qi >> 3) * 56 + 16 * h;
    u32x2 a = *(const u32x2*)qp, b = *(const u32x2*)(qp + 8), c = *(const u32x2*)(qp + 32), d = *(const u32x2*)(qp + (h ? 32 : 40));
    if (h) d = (u32x2){0u, 0u};
    u32x4 t0 = {a[0], a[1], b[0], b[1]}, t1 = {c[0], c[1], d[0], d[1]};
    q0 = __builtin_bit_cast(bf16x8, t0); q1 = __builtin_bit_cast(bf16x8, t1);
  }
  f32x16 sa0, sa1, sb0, sb1;
  {
    const char* kb = smem + kh * 4096 + lane * 16;
    bf16x8 k00 = lds_ld16(kb), k01 = lds_ld16(kb + 1024), k10 = lds_ld16(kb + 2048), k11 = lds_ld16(kb + 3072);
    sa0 = MFMA32(k00, q0, (f32x16){}); sa1 = MFMA32(k10, q0, (f32x16){});
    sa0 = MFMA32(k01, q1, sa0); sa1 = MFMA32(k11, q1, sa1);
  }
  float sbound;
  {
    float n2 = 0.f;
#pragma unroll
    for (int i = 0; i < 8; ++i) { n2 = fmaf((float)q0[i], (float)q0[i], n2); n2 = fmaf((float)q1[i], (float)q1[i], n2); }
    n2 = half_sum(n2);
    const float kn2 = wave_max_nonneg(((const float*)(smem + K2_POFF))[lane]);
    sbound = sqrtf(n2 * kn2) * 1.01f + 1.0f;
  }
  bool fast = false;
  f32x16 o = {};
  float mref = 0.f;
  auto iter = [&](f32x16& c0, f32x16& c1, f32x16& n0, f32x16& n1, const int s) __attribute__((always_inline)) {
    barrier_raw();
    const char* kb = smem + ((s + 1) & 3) * 8192 + kh * 4096 + lane * 16;
    const char* vb = smem + K2_VOFF + (s & 3) * 8192 + kh * 4096 + lane * 16;
    bf16x8 k00 = lds_ld16(kb), k01 = lds_ld16(kb + 1024), k10 = lds_ld16(kb + 2048), k11 = lds_ld16(kb + 3072);
    bf16x8 v00 = lds_ld16(vb), v01 = lds_ld16(vb + 1024), v10 = lds_ld16(vb + 2048), v11 = lds_ld16(vb + 3072);
    __builtin_amdgcn_sched_barrier(0);
    if (!fast) {
      float pmax = -3.0e38f;
#pragma unroll
      for (int i = 0; i < 16; i += 2) pmax = fmaxf(fmaxf(pmax, c0[i]), c0[i + 1]);
#pragma unroll
      for (int i = 0; i < 16; i += 2) pmax = fmaxf(fmaxf(pmax, c1[i]), c1[i + 1]);
      {
        auto rr = __builtin_amdgcn_permlane32_swap(__float_as_uint(pmax), __float_as_uint(pmax), false, false);
        pmax = fmaxf(fmaxf(pmax, __uint_as_float(rr[0])), __uint_as_float(rr[1]));
      }
      if (s == 0 || !__all(pmax <= THR_L2)) {
        const float mnew = rbf(mref + (s == 0 ? pmax : fmaxf(pmax, 0.f)));
        const float delta = mnew - mref;
        mref = mnew;
        if (s != 0) {
          const float alpha = __builtin_amdgcn_exp2f(-delta);
#pragma unroll
          for (int i = 0; i < 16; ++i) o[i] *= alpha;
        }
#pragma unroll
        for (int i = 0; i < 16; ++i) { c0[i] -= delta; c1[i] -= delta; }
        if (h) q1[4] = (__bf16)(-mnew);
      }
      if (s == 0) fast = __all(sbound - mref <= 64.0f);
    }
#pragma unroll
    for (int i = 0; i < 8; ++i) c0[i] = __builtin_amdgcn_exp2f(c0[i]);
    __builtin_amdgcn_sched_barrier(0);
    n0 = MFMA32(k00, q0, (f32x16){}); n1 = MFMA32(k10, q0, (f32x16){});
#pragma unroll
    for (int i = 8; i < 16; ++i) c0[i] = __builtin_amdgcn_exp2f(c0[i]);
    u32x4 p00 = {pk2(c0[0], c0[1]), pk2(c0[2], c0[3]), pk2(c0[4], c0[5]), pk2(c0[6], c0[7])};
    u32x4 p01 = {pk2(c0[8], c0[9]), pk2(c0[10], c0[11]), pk2(c0[12], c0[13]), pk2(c0[14], c0[15])};
    __builtin_amdgcn_sched_barrier(0);
    n0 = MFMA32(k01, q1, n0); n1 = MFMA32(k11, q1, n1);
#pragma unroll
    for (int i = 0; i < 8; ++i) c1[i] = __builtin_amdgcn_exp2f(c1[i]);
    __builtin_amdgcn_sched_barrier(0);
    o = MFMA32(v00, __builtin_bit_cast(bf16x8, p00), o);
    o = MFMA32(v01, __builtin_bit_cast(bf16x8, p01), o);
#pragma unroll
    for (int i = 8; i < 16; ++i) c1[i] = __builtin_amdgcn_exp2f(c1[i]);
    u32x4 p10 = {pk2(c1[0], c1[1]), pk2(c1[2], c1[3]), pk2(c1[4], c1[5]), pk2(c1[6], c1[7])};
    u32x4 p11 = {pk2(c1[8], c1[9]), pk2(c1[10], c1[11]), pk2(c1[12], c1[13]), pk2(c1[14], c1[15])};
    __builtin_amdgcn_sched_barrier(0);
    o = MFMA32(v10, __builtin_bit_cast(bf16x8, p10), o);
    o = MFMA32(v11, __builtin_bit_cast(bf16x8, p11), o);
  };
#pragma unroll
  for (int s = 0; s < 16; s += 2) {
    iter(sa0, sa1, sb0, sb1, s);
    iter(sb0, sb1, sa0, sa1, s + 1);
  }
  barrier_raw();
  char* outt = smem + 40960;
  {
    char* red = smem + (qg * 2 + kh) * 5120 + lane * 16;
#pragma unroll
    for (int g = 0; g < 4; ++g) *(f32x4*)(red + g * 1024) = (f32x4){o[4 * g], o[4 * g + 1], o[4 * g + 2], o[4 * g + 3]};
    *(float*)(smem + (qg * 2 + kh) * 5120 + 4096 + lane * 4) = mref;
  }
  {
    const int pl = w, hd = lane >> 2, sub = lane & 3;
    const char* qb = smem + K2_ROFF + pl * RS + 896 + hd * 56;
    const char* kb = smem + K2_KVOFF + (14 + pl - 2 * sub) * RS + hd * 56;
    float acc0 = 0.f, acc1 = 0.f;
#pragma unroll
    for (int i = 0; i < 7; ++i) {
      const u32x2 qv = *(const u32x2*)(qb + i * 8), ka = *(const u32x2*)(kb + i * 8), kc = *(const u32x2*)(kb - 8 * RS + i * 8);
      const float q0f = bflo(qv[0]), q1f = bfhi(qv[0]), q2f = bflo(qv[1]), q3f = bfhi(qv[1]);
      acc0 = fmaf(q0f, bflo(ka[0]), acc0); acc0 = fmaf(q1f, bfhi(ka[0]), acc0);
      acc0 = fmaf(q2f, bflo(ka[1]), acc0); acc0 = fmaf(q3f, bfhi(ka[1]), acc0);
      acc1 = fmaf(q0f, bflo(kc[0]), acc1); acc1 = fmaf(q1f, bfhi(kc[0]), acc1);
      acc1 = fmaf(q2f, bflo(kc[1]), acc1); acc1 = fmaf(q3f, bfhi(kc[1]), acc1);
    }
    float lv0 = rbf(rbf(acc0) * SCALE_), lv1 = rbf(rbf(acc1) * SCALE_);
    if (pos0 + pl - 2 * sub < 0) lv0 = rbf(-1e9f);
    if (pos0 + pl - 2 * sub - 8 < 0) lv1 = rbf(-1e9f);
    float mx = fmaxf(lv0, lv1);
    mx = fmaxf(mx, dpp_f<0xB1>(mx));
    mx = fmaxf(mx, dpp_f<0x4E>(mx));
    const float e0 = __expf(lv0 - mx), e1 = __expf(lv1 - mx);
    float sum = e0 + e1;
    sum += dpp_f<0xB1>(sum);
    sum += dpp_f<0x4E>(sum);
    const float inv = 1.0f / sum;
    const float p0 = rbf(e0 * inv), p1 = rbf(e1 * inv);
    float pw[8];
    pw[0] = dpp_f<0x00>(p0); pw[1] = dpp_f<0x55>(p0); pw[2] = dpp_f<0xAA>(p0); pw[3] = dpp_f<0xFF>(p0);
    pw[4] = dpp_f<0x00>(p1); pw[5] = dpp_f<0x55>(p1); pw[6] = dpp_f<0xAA>(p1); pw[7] = dpp_f<0xFF>(p1);
    const int ca = hd * 56 + sub * 8, cb = hd * 56 + (sub < 3 ? sub + 4 : sub) * 8;
    float oa[4] = {0.f, 0.f, 0.f, 0.f}, ob[4] = {0.f, 0.f, 0.f, 0.f};
#pragma unroll
    for (int ww = 0; ww < 8; ++ww) {
      const char* vr = smem + K2_KVOFF + (14 + pl - 2 * ww) * RS + 896;
      const u32x2 va = *(const u32x2*)(vr + ca), vb = *(const u32x2*)(vr + cb);
      oa[0] = fmaf(pw[ww], bflo(va[0]), oa[0]); oa[1] = fmaf(pw[ww], bfhi(va[0]), oa[1]);
      oa[2] = fmaf(pw[ww], bflo(va[1]), oa[2]); oa[3] = fmaf(pw[ww], bfhi(va[1]), oa[3]);
      ob[0] = fmaf(pw[ww], bflo(vb[0]), ob[0]); ob[1] = fmaf(pw[ww], bfhi(vb[0]), ob[1]);
      ob[2] = fmaf(pw[ww], bflo(vb[1]), ob[2]); ob[3] = fmaf(pw[ww], bfhi(vb[1]), ob[3]);
    }
    *(u32x2*)(smem + K2_BOUT + pl * 896 + ca) = (u32x2){pk2(oa[0], oa[1]), pk2(oa[2], oa[3])};
    if (sub < 3) *(u32x2*)(smem + K2_BOUT + pl * 896 + cb) = (u32x2){pk2(ob[0], ob[1]), pk2(ob[2], ob[3])};
  }
  WAIT_LGKM0();
  barrier_raw();
  {
    const char* base = smem + qg * 2 * 5120;
    const float m0_ = *(const float*)(base + 4096 + lane * 4), m1_ = *(const float*)(base + 5120 + 4096 + lane * 4);
    const float l0 = *(const float*)(base + 3 * 1024 + (32 + r) * 16), l1 = *(const float*)(base + 5120 + 3 * 1024 + (32 + r) * 16);
    const float mn = fmaxf(m0_, m1_);
    float a0 = __builtin_amdgcn_exp2f(m0_ - mn), a1 = __builtin_amdgcn_exp2f(m1_ - mn);
    const float inv = 1.0f / (l0 * a0 + l1 * a1);
    a0 *= inv; a1 *= inv;
    const int qi = qg * 32 + r;
#pragma unroll
    for (int gg = 0; gg < 2; ++gg) {
      const int g = 2 * kh + gg;
      const f32x4 acc = *(const f32x4*)(base + g * 1024 + lane * 16) * a0 + *(const f32x4*)(base + 5120 + g * 1024 + lane * 16) * a1;
      if (!(g == 3 && h == 1))
        *(u32x2*)(outt + (qi & 7) * 896 + (qi >> 3) * 56 + 8 * h + 16 * g) = (u32x2){pk2(acc[0], acc[1]), pk2(acc[2], acc[3])};
    }
  }
  WAIT_LGKM0();
  barrier_raw();
  if (lane < 56) {
    char* dst = ws + WS_OAB + (size_t)(pos0 + w) * 1792 + lane * 16;
    st16_wt(dst, *(const u32x4*)(outt + w * 896 + lane * 16));
    st16_wt(dst + 896, *(const u32x4*)(smem + K2_BOUT + w * 896 + lane * 16));
  }
}

constexpr int K3_STAGE = 32768;
constexpr int K3_LDS = 4 * K3_STAGE;

__global__ __launch_bounds__(512) void k3_out(const bf16_t* __restrict__ woa, const bf16_t* __restrict__ wob,
                                              const char* __restrict__ ws, bf16_t* __restrict__ out) {
  extern __shared__ __attribute__((aligned(16))) char smem[];
  const int tid = threadIdx.x, lane = tid & 63, w = __builtin_amdgcn_readfirstlane(tid >> 6);
  const int r = lane & 31, h = lane >> 5;
  const int bid = blockIdx.x;
  const int xcd = bid & 7, idx = bid >> 3;
  const int mt = xcd * 4 + idx / 7, nt = idx % 7;
  const int m0 = mt * 64, n0 = nt * 64;
  const int br = w >> 2, wl = w & 3;
  const bf16_t* oab = (const bf16_t*)(ws + WS_OAB);
  const bf16_t* wo = br ? wob : woa;
  const bf16_t* osrc[2];
  const bf16_t* wsrc[2];
#pragma unroll
  for (int i = 0; i < 2; ++i) {
    int row = (wl * 2 + i) * 8 + (lane >> 3), slot = lane & 7;
    osrc[i] = oab + (size_t)(m0 + row) * 896 + br * 448 + ((slot ^ ((row >> 1) & 7)) * 8);
    wsrc[i] = wo + (size_t)row * 448 + n0 + ((slot ^ (((row >> 1) & 1) << 2)) * 8);
  }
  auto issue = [&](int s) {
    const unsigned base = lds_off(smem) + (s & 3) * K3_STAGE + br * 16384;
#pragma unroll
    for (int i = 0; i < 2; ++i) {
      glds16(osrc[i] + s * 64, base + (wl * 2 + i) * 1024);
      glds16(wsrc[i] + (size_t)s * 64 * 448, base + 8192 + (wl * 2 + i) * 1024);
    }
  };
  issue(0); issue(1); issue(2);
  const int wn = wl & 1, wm = wl >> 1;
  const int orow = wm * 32 + r;
  const int g16 = lane >> 4, q4 = (lane & 15) >> 2, p4 = lane & 3;
  const int trh = g16 >> 1, trb = g16 & 1;
  const int wchunk = wn * 4 + trb * 2 + (p4 >> 1);
  f32x16 acc = {};
#pragma unroll
  for (int s = 0; s < 7; ++s) {
    if (s < 5) WAIT_VM(8); else if (s == 5) WAIT_VM(4); else WAIT_VM(0);
    barrier_raw();
    if (s + 3 < 7) issue(s + 3);
    const char* ob = smem + (s & 3) * K3_STAGE + br * 16384;
    const char* wb = ob + 8192;
#pragma unroll
    for (int ks = 0; ks < 4; ++ks) {
      bf16x8 b = lds_ld16(ob + orow * 128 + (((2 * ks + h) ^ ((orow >> 1) & 7)) * 16));
      int k0 = ks * 16 + trh * 8 + q4;
      s16x4 a0 = lds_tr(wb + k0 * 128 + ((wchunk ^ (((k0 >> 1) & 1) << 2)) * 16) + (p4 & 1) * 8);
      s16x4 a1 = lds_tr(wb + (k0 + 4) * 128 + ((wchunk ^ (((k0 >> 1) & 1) << 2)) * 16) + (p4 & 1) * 8);
      acc = MFMA32(cat_tr(a0, a1), b, acc);
    }
  }
  barrier_raw();
  float* red = (float*)smem + wl * (16 * 64);
  if (br == 1) {
#pragma unroll
    for (int i = 0; i < 16; ++i) red[i * 64 + lane] = acc[i];
  }
  WAIT_LGKM0();
  barrier_raw();
  char* ot = smem + 32768;
  if (br == 0) {
    const int m = m0 + orow;
    const float g = ((const float*)(ws + WS_G))[m];
    const float omg = rbf(1.0f - g);
#pragma unroll
    for (int gq = 0; gq < 4; ++gq) {
      float res[4];
#pragma unroll
      for (int e = 0; e < 4; ++e) {
        float a = rbf(acc[4 * gq + e]);
        float b = rbf(red[(4 * gq + e) * 64 + lane]);
        res[e] = rbf(g * a) + rbf(omg * b);
      }
      u32x2 v = {pk2(res[0], res[1]), pk2(res[2], res[3])};
      *(u32x2*)(ot + orow * 144 + (wn * 32 + 8 * gq + 4 * h) * 2) = v;
    }
  }
  WAIT_LGKM0();
  barrier_raw();
  {
    const int row = w * 8 + (lane >> 3), c = lane & 7;
    u32x4 v = *(const u32x4*)(ot + row * 144 + c * 16);
    st16_wt(out + (size_t)(m0 + row) * D_ + n0 + c * 8, v);
  }
}

extern "C" void kernel_launch(void* const* d_in, const int* in_sizes, int n_in, void* d_out, int out_size, void* d_ws,
                              size_t ws_size, hipStream_t stream) {
  const bf16_t* x = (const bf16_t*)d_in[0];
  const bf16_t* wq = (const bf16_t*)d_in[1];
  const bf16_t* wk = (const bf16_t*)d_in[2];
  const bf16_t* wv = (const bf16_t*)d_in[3];
  const bf16_t* woa = (const bf16_t*)d_in[4];
  const bf16_t* wqkv = (const bf16_t*)d_in[6];
  const bf16_t* wob = (const bf16_t*)d_in[7];
  const bf16_t* gw = (const bf16_t*)d_in[8];
  char* ws = (char*)d_ws;
  (void)hipFuncSetAttribute((const void*)k1_proj, hipFuncAttributeMaxDynamicSharedMemorySize, K1_LDS);
  (void)hipFuncSetAttribute((const void*)k2_attn, hipFuncAttributeMaxDynamicSharedMemorySize, K2_LDS);
  (void)hipFuncSetAttribute((const void*)k3_out, hipFuncAttributeMaxDynamicSharedMemorySize, K3_LDS);
  hipLaunchKernelGGL(k1_proj, dim3(256), dim3(512), K1_LDS, stream, x, wq, wk, wv, wqkv, gw, ws);
  hipLaunchKernelGGL(k2_attn, dim3(256), dim3(768), K2_LDS, stream, ws);
  hipLaunchKernelGGL(k3_out, dim3(224), dim3(512), K3_LDS, stream, woa, wob, (const char*)ws, (bf16_t*)d_out);
}
```
